```python
import torch

def _softmax(x, axis=-1):
    return torch.softmax(x, dim=axis)
def _gelu(x):
    return torch.nn.functional.gelu(x, approximate="tanh")
def _silu(x):
    return torch.nn.functional.silu(x)
def _sigmoid(x):
    return torch.sigmoid(x)
def _layernorm(x, axis=-1, eps=1e-5):
    m = x.mean(dim=axis, keepdim=True)
    v = x.var(dim=axis, keepdim=True, unbiased=False)
    return (x - m) / torch.sqrt(v + eps)
def _scatter_add(tgt, idx, src, axis=0):
    return tgt.clone().index_add_(axis, idx, src)
def _segment_sum(src, seg, n):
    out = torch.zeros((n,) + src.shape[1:], dtype=src.dtype)
    return out.index_add_(0, seg, src)
def _pad(x, pw):
    flat = tuple(v for pair in reversed(pw) for v in pair)
    return torch.nn.functional.pad(x, flat)

kernel_name = 'interleave_layers__ssm_conv_h3__ssm_conv_s4d__prompt'

def setup_inputs():

    _w = (lambda a: (lambda t: t.to(torch.bfloat16) if t.is_floating_point() else t)(torch.as_tensor(a)))
    def _A_setup():
        B, S, D = 1, 2048, 512
        ks = 4
        return {
            "q": _w((lambda *s: torch.randn(*s, dtype=torch.bfloat16) * 0.05)(B, S, D)),
            "k": _w((lambda *s: torch.randn(*s, dtype=torch.bfloat16) * 0.4)(B, S, D)),
            "v": _w((lambda *s: torch.randn(*s, dtype=torch.bfloat16) * 0.4)(B, S, D)),
            "shift_k": _w((lambda *s: torch.randn(*s, dtype=torch.bfloat16) * 0.4)(D, ks) * 0.5),
            "ssm_k": _w((lambda *s: torch.randn(*s, dtype=torch.bfloat16) * 0.4)(D, S) * (1.0 / S ** 0.5)),
            "ssm_D": _w((lambda *s: torch.randn(*s, dtype=torch.bfloat16) * 0.4)(D)),
        }
    _A_in = _A_setup()

    def _B_setup():
        Bsz, L, D, N = 1, 2048, 512, 128
        return {
            "u": _w((lambda *s: torch.randn(*s, dtype=torch.bfloat16) * 0.05)(Bsz, L, D)),
            "log_A_real": _w((lambda *s: torch.randn(*s, dtype=torch.bfloat16) * 6.4)(D, N) * 0.5),
            "A_imag": _w((lambda *s: torch.randn(*s, dtype=torch.bfloat16) * 0.05)(D, N) * 3.14),
            "C_real": _w((lambda *s: torch.randn(*s, dtype=torch.bfloat16) * 0.1)(D, N) / N ** 0.5),
            "C_imag": _w((lambda *s: torch.randn(*s, dtype=torch.bfloat16) * 0.1)(D, N) / N ** 0.5),
            "log_dt": _w((lambda *s: torch.randn(*s, dtype=torch.bfloat16) * 0.05)(D) * 0.5 - 4.0),
            "D_skip": _w((lambda *s: torch.randn(*s, dtype=torch.bfloat16) * 0.05)(D)),
        }
    _B_in = _B_setup()

    return {
        "A_q": _A_in["q"],
        "A_k": _A_in["k"],
        "A_v": _A_in["v"],
        "A_shift_k": _A_in["shift_k"],
        "A_ssm_k": _A_in["ssm_k"],
        "A_ssm_D": _A_in["ssm_D"],
        "B_u": _B_in["u"],
        "B_log_A_real": _B_in["log_A_real"],
        "B_A_imag": _B_in["A_imag"],
        "B_C_real": _B_in["C_real"],
        "B_C_imag": _B_in["C_imag"],
        "B_log_dt": _B_in["log_dt"],
        "B_D_skip": _B_in["D_skip"],
    }

def reference(A_q, A_k, A_v, A_shift_k, A_ssm_k, A_ssm_D, B_u, B_log_A_real, B_A_imag, B_C_real, B_C_imag, B_log_dt, B_D_skip):
    def _A_ref(q, k, v, shift_k, ssm_k, ssm_D):
        B, S, D = q.shape
        ks = shift_k.shape[-1]
        k_pad = _pad(k, ((0, 0), (ks - 1, 0), (0, 0)))
        k_shift = torch.zeros_like(k)
        for t in range(ks):
            k_shift = k_shift + k_pad[:, t:t + S, :] * shift_k[:, ks - 1 - t][None, None, :]
        kv = k_shift * v
        L = 2 * S
        kv_pad = _pad(kv, ((0, 0), (0, S), (0, 0)))
        kf_pad = _pad(ssm_k, ((0, 0), (0, S)))
        kv_f = (lambda x, axes=(-2,-1): torch.fft.rfft2(x.float(), dim=axes))(kv_pad[:, None, :, :], axes=(1, 2))
        kf_f = (lambda x, axes=(-2,-1): torch.fft.rfft2(x.float(), dim=axes))(kf_pad[:, None, :], axes=(1, 2))
        kf_f = (lambda x, *d: x.permute(*d))(kf_f, 1, 2, 0)
        y_f = kv_f * kf_f[None, :, :, :]
        y = (lambda x, s, axes=(-2,-1): torch.fft.irfft2(x, s=s, dim=axes).to(torch.bfloat16))(y_f, (1, L), axes=(1, 2))[:, 0, :S, :]
        y = y + kv * ssm_D[None, None, :]
        return q * y

    def _B_ref(u, log_A_real, A_imag, C_real, C_imag, log_dt, D_skip):
        L = u.shape[1]
        dt = torch.exp(log_dt)
        A = -torch.exp(log_A_real) + 1j * A_imag
        C = (C_real + 1j * C_imag) * dt[:, None]
        pos = (lambda x, dt: x.to(dt))(torch.arange(L), log_dt.dtype)
        P = torch.exp((A * dt[:, None])[:, :, None] * pos[None, None, :])
        K = 2.0 * torch.einsum("dn,dnl->dl", C, P).real
        L2 = 2 * L
        u_t = (lambda x, *d: x.permute(*d))(u, 0, 2, 1)
        Kf = (lambda x, *a, **k: torch.fft.rfft(x.float(), *a, **k))(K, L2)
        Uf = (lambda x, *a, **k: torch.fft.rfft(x.float(), *a, **k))(u_t, L2)
        y = (lambda x, *a, **k: torch.fft.irfft(x, *a, **k).to(torch.bfloat16))(Uf * Kf[None, :, :], L2)[:, :, :L]
        y = (lambda x, *d: x.permute(*d))(y, 0, 2, 1)
        return y + u * D_skip[None, None, :]

    x = A_q
    x = x + _A_ref(_layernorm(x, axis=-1), A_k, A_v, A_shift_k, A_ssm_k, A_ssm_D)
    x = x + _B_ref(_layernorm(x, axis=-1), B_log_A_real, B_A_imag, B_C_real, B_C_imag, B_log_dt, B_D_skip)
    x = x + _A_ref(_layernorm(x, axis=-1), A_k, A_v, A_shift_k, A_ssm_k, A_ssm_D)
    x = x + _B_ref(_layernorm(x, axis=-1), B_log_A_real, B_A_imag, B_C_real, B_C_imag, B_log_dt, B_D_skip)
    return x
```

```cpp
#include <hip/hip_runtime.h>
#include <hip/hip_bf16.h>

typedef unsigned short u16;
typedef unsigned u32;
typedef u32 v4u __attribute__((ext_vector_type(4)));
typedef u32 v2u __attribute__((ext_vector_type(2)));
typedef float f4 __attribute__((ext_vector_type(4)));
typedef float f2 __attribute__((ext_vector_type(2)));
typedef __bf16 bf8 __attribute__((ext_vector_type(8)));
typedef __bf16 bf2 __attribute__((ext_vector_type(2)));
typedef _Float16 h8 __attribute__((ext_vector_type(8)));
typedef _Float16 h2 __attribute__((ext_vector_type(2)));
typedef short s4 __attribute__((ext_vector_type(4)));
#define LAS __attribute__((address_space(3)))

constexpr int SEQ = 2048, DM = 512, NS = 128;
constexpr u32 SENT = 0xFFFFFFFFu;
constexpr size_t MB2 = 2u * 1024u * 1024u;
constexpr size_t WS_KVT = 0, WS_KVD = MB2, WS_BOX = 2 * MB2;
constexpr size_t WS_STAMP = 8 * MB2;
constexpr float USCALE = 16.0f;

constexpr int KC_S = 4160;
constexpr int L_KC = 0;
constexpr int U_S = 4672;
constexpr int L_U = L_KC + 2 * 8 * KC_S;
constexpr int L_P = L_U + 2 * U_S;
constexpr int L_TI = L_P + 32768;
constexpr int L_TO = L_TI + 8 * 1040;
constexpr int L_PA = L_TO + 8 * 1088;
constexpr int L_PH = L_PA + 4096;
constexpr int L_KCMP = L_PH + 1024;
constexpr int L_MX = L_KCMP + 2 * 1312;
constexpr int L_END = L_MX + 64;

__device__ __forceinline__ float bf2f(u32 h) { return __builtin_bit_cast(float, h << 16); }
__device__ __forceinline__ float lo2f(u32 d) { return __builtin_bit_cast(float, d << 16); }
__device__ __forceinline__ float hi2f(u32 d) { return __builtin_bit_cast(float, d & 0xFFFF0000u); }
__device__ __forceinline__ float rb(float x) { f2 t = {0.f, x}; return __builtin_bit_cast(float, __builtin_convertvector(t, bf2)); }
__device__ __forceinline__ u32 pk_bf(float a, float b) { f2 t = {a, b}; return __builtin_bit_cast(u32, __builtin_convertvector(t, bf2)); }
__device__ __forceinline__ u32 pk_h(float a, float b) { f2 t = {a, b}; return __builtin_bit_cast(u32, __builtin_convertvector(t, h2)); }
__device__ __forceinline__ u32 clampS(u32 d) { return d < 0xFFFFFFFEu ? d : 0xFFFFFFFEu; }
__device__ __forceinline__ void unpack8(v4u d, float* o) {
    o[0] = lo2f(d.x); o[1] = hi2f(d.x); o[2] = lo2f(d.y); o[3] = hi2f(d.y);
    o[4] = lo2f(d.z); o[5] = hi2f(d.z); o[6] = lo2f(d.w); o[7] = hi2f(d.w);
}
template <int CTRL> __device__ __forceinline__ float dppf(float v) {
    return __builtin_bit_cast(float, __builtin_amdgcn_update_dpp(0, __builtin_bit_cast(int, v), CTRL, 0xf, 0xf, true));
}
__device__ __forceinline__ float rdl(float v, int l) { return __builtin_bit_cast(float, __builtin_amdgcn_readlane(__builtin_bit_cast(int, v), l)); }
__device__ __forceinline__ float wave_sum(float v) {
    v += dppf<0xB1>(v); v += dppf<0x4E>(v); v += dppf<0x141>(v); v += dppf<0x140>(v);
    return (rdl(v, 0) + rdl(v, 16)) + (rdl(v, 32) + rdl(v, 48));
}
__device__ __forceinline__ float wave_max(float v) {
    v = fmaxf(v, dppf<0xB1>(v)); v = fmaxf(v, dppf<0x4E>(v)); v = fmaxf(v, dppf<0x141>(v)); v = fmaxf(v, dppf<0x140>(v));
    return fmaxf(fmaxf(rdl(v, 0), rdl(v, 16)), fmaxf(rdl(v, 32), rdl(v, 48)));
}
__device__ __forceinline__ void ln_row(const float* x, float* u) {
    float s = ((x[0] + x[1]) + (x[2] + x[3])) + ((x[4] + x[5]) + (x[6] + x[7]));
    const float mean = wave_sum(s) * (1.0f / DM);
    float q = 0.f;
#pragma unroll
    for (int i = 0; i < 8; ++i) { const float d = x[i] - mean; q = fmaf(d, d, q); }
    const float var = wave_sum(q) * (1.0f / DM);
    const float mb = rb(mean);
    const float sd = rb(__builtin_amdgcn_sqrtf(rb(rb(var) + 1e-5f)));
    const float rs = __builtin_amdgcn_rcpf(sd);
#pragma unroll
    for (int i = 0; i < 8; ++i) u[i] = rb(rb(x[i] - mb) * rs);
}

__global__ void __launch_bounds__(256) k0_prep(const u16* __restrict__ kin, const u16* __restrict__ vin, const u16* __restrict__ shk,
                                               const u16* __restrict__ ssmD, unsigned char* __restrict__ ws) {
    __shared__ u32 T[64 * 33];
    const int b = blockIdx.x, t = threadIdx.x;
    const int cb = b & 7, rbk = b >> 3;
    const int cc = t & 7, rp = t >> 3;
    const int c0 = cb * 64 + cc * 8, r = rbk * 64 + rp * 2;
    v4u kk[5], vv[2], sw[4], dd;
#pragma unroll
    for (int i = 0; i < 5; ++i) {
        const int rr = r - 3 + i;
        kk[i] = (rr >= 0) ? *(const v4u*)(kin + (size_t)rr * DM + c0) : (v4u){0u, 0u, 0u, 0u};
    }
    vv[0] = *(const v4u*)(vin + (size_t)r * DM + c0);
    vv[1] = *(const v4u*)(vin + (size_t)(r + 1) * DM + c0);
#pragma unroll
    for (int i = 0; i < 4; ++i) sw[i] = *(const v4u*)(shk + (size_t)c0 * 4 + i * 8);
    dd = *(const v4u*)(ssmD + c0);
    {
        const __amdgpu_buffer_rsrc_t rs = __builtin_amdgcn_make_buffer_rsrc(ws + WS_BOX, 0, (int)MB2, 0x00020000);
        const v4u s = {SENT, SENT, SENT, SENT};
        __builtin_amdgcn_raw_buffer_store_b128(s, rs, b * 8192 + t * 16, 0, 16);
        __builtin_amdgcn_raw_buffer_store_b128(s, rs, b * 8192 + 4096 + t * 16, 0, 16);
    }
    float kf[5][8], vf[2][8], df[8], w[8][4];
#pragma unroll
    for (int i = 0; i < 5; ++i) unpack8(kk[i], kf[i]);
    unpack8(vv[0], vf[0]); unpack8(vv[1], vf[1]); unpack8(dd, df);
#pragma unroll
    for (int i = 0; i < 4; ++i) { float tmp[8]; unpack8(sw[i], tmp);
#pragma unroll
        for (int j = 0; j < 8; ++j) w[2 * i + (j >> 2)][j & 3] = tmp[j]; }
    float kv[2][8], kd[2][8];
#pragma unroll
    for (int rr = 0; rr < 2; ++rr)
#pragma unroll
        for (int c = 0; c < 8; ++c) {
            float a = rb(kf[rr][c] * w[c][3]);
            a = rb(a + rb(kf[rr + 1][c] * w[c][2]));
            a = rb(a + rb(kf[rr + 2][c] * w[c][1]));
            a = rb(a + rb(kf[rr + 3][c] * w[c][0]));
            kv[rr][c] = rb(a * vf[rr][c]);
            kd[rr][c] = kv[rr][c] * df[c];
        }
    u16* kvD = (u16*)(ws + WS_KVD);
#pragma unroll
    for (int rr = 0; rr < 2; ++rr) {
        v4u o = {pk_bf(kd[rr][0], kd[rr][1]), pk_bf(kd[rr][2], kd[rr][3]), pk_bf(kd[rr][4], kd[rr][5]), pk_bf(kd[rr][6], kd[rr][7])};
        *(v4u*)(kvD + (size_t)(r + rr) * DM + c0) = o;
    }
#pragma unroll
    for (int c = 0; c < 8; ++c) T[(cc * 8 + c) * 33 + rp] = pk_bf(kv[0][c], kv[1][c]);
    __syncthreads();
    const int ch = t >> 2, qt = t & 3;
    u32 o[8];
#pragma unroll
    for (int i = 0; i < 8; ++i) o[i] = T[ch * 33 + qt * 8 + i];
    u16* kvT = (u16*)(ws + WS_KVT) + (size_t)(cb * 64 + ch) * SEQ + rbk * 64 + qt * 16;
    *(v4u*)(kvT) = (v4u){o[0], o[1], o[2], o[3]};
    *(v4u*)(kvT + 8) = (v4u){o[4], o[5], o[6], o[7]};
}

__device__ __forceinline__ bool has_sent(v4u v) { return v.x == SENT || v.y == SENT || v.z == SENT || v.w == SENT; }

__device__ __forceinline__ u32 ld1_dev(const unsigned char* p) {
    u32 v; asm volatile("global_load_dword %0, %1, off sc1\n\ts_waitcnt vmcnt(0)" : "=v"(v) : "v"(p) : "memory"); return v;
}
__device__ __forceinline__ v4u ld4_dev(const unsigned char* p) {
    v4u v; asm volatile("global_load_dwordx4 %0, %1, off sc1\n\ts_waitcnt vmcnt(0)" : "=v"(v) : "v"(p) : "memory"); return v;
}
__device__ __forceinline__ v4u box_get(const unsigned char* base, u32 off) {
    const unsigned char* cp = base + __builtin_amdgcn_readfirstlane(off);
#pragma unroll 1
    for (u32 sp = 0; sp < (1u << 17); ++sp) {
        const u32 c = ld1_dev(cp);
        if (__builtin_amdgcn_readfirstlane(c) != SENT) break;
        __builtin_amdgcn_s_sleep(1);
    }
    v4u v;
#pragma unroll 1
    for (u32 sp = 0; sp < (1u << 17); ++sp) {
        v = ld4_dev(base + off);
        if (!__any(has_sent(v))) break;
    }
    return v;
}

__device__ __forceinline__ void write_copies(LAS unsigned char* kc  , v4u prv, v4u cur) {
    const u32 d[8] = {prv.x, prv.y, prv.z, prv.w, cur.x, cur.y, cur.z, cur.w};
    u32 Ro[8], Re[8];
#pragma unroll
    for (int i = 0; i < 8; ++i) Ro[i] = __builtin_amdgcn_alignbit(d[i], d[i], 16);
#pragma unroll
    for (int i = 1; i < 8; ++i) Re[i] = (d[i] & 0xFFFFu) | (d[i - 1] & 0xFFFF0000u);
    Re[0] = 0;
#pragma unroll
    for (int s = 0; s < 8; ++s) {
        const int i0 = (8 + s) >> 1;
        v4u o;
        if (s & 1) o = (v4u){Ro[i0], Ro[i0 - 1], Ro[i0 - 2], Ro[i0 - 3]};
        else o = (v4u){Re[i0], Re[i0 - 1], Re[i0 - 2], Re[i0 - 3]};
        *(LAS v4u*)(kc + s * KC_S) = o;
    }
}

template <bool BF> __device__ __forceinline__ f4 mm(v4u a, v4u b, f4 c) {
    if constexpr (BF) return __builtin_amdgcn_mfma_f32_16x16x32_bf16(__builtin_bit_cast(bf8, a), __builtin_bit_cast(bf8, b), c, 0, 0, 0);
    else return __builtin_amdgcn_mfma_f32_16x16x32_f16(__builtin_bit_cast(h8, a), __builtin_bit_cast(h8, b), c, 0, 0, 0);
}

__device__ __forceinline__ void load_afrags(v4u (&a)[4][8], LAS const unsigned char* abase) {
#pragma unroll
    for (int p = 0; p < 8; ++p)
#pragma unroll
        for (int ri = 0; ri < 4; ++ri) a[ri][p] = *(LAS const v4u*)(abase + 512 * p + 64 * ri);
}

template <bool BF, int TH>
__device__ __forceinline__ void conv_core(const v4u (&a)[4][8], LAS const unsigned char* bbase, LAS unsigned char* pbase) {
    constexpr int TL[4] = {TH ? 1 : 0, TH ? 2 : 3, TH ? 5 : 4, TH ? 6 : 7};
    f4 acc[4] = {{0.f, 0.f, 0.f, 0.f}, {0.f, 0.f, 0.f, 0.f}, {0.f, 0.f, 0.f, 0.f}, {0.f, 0.f, 0.f, 0.f}};
#pragma unroll
    for (int ri = 0; ri < 4; ++ri) {
        v4u bq[8];
#pragma unroll
        for (int q = 0; q <= TL[3]; ++q) bq[q] = *(LAS const v4u*)(bbase + 512 * q + 64 * (3 - ri));
#pragma unroll
        for (int q = 0; q <= TL[3]; ++q)
#pragma unroll
            for (int ti = 0; ti < 4; ++ti)
                if (TL[ti] - q >= 0) acc[ti] = mm<BF>(a[ri][TL[ti] - q], bq[q], acc[ti]);
    }
#pragma unroll
    for (int ti = 0; ti < 4; ++ti) *(LAS f4*)(pbase + TL[ti] * 1024) = acc[ti];
}

#define STAMP(i) do {} while (0)

__global__ void __launch_bounds__(512, 2)
k1_main(const u16* __restrict__ qin, const u16* __restrict__ ssmk, const u16* __restrict__ lar, const u16* __restrict__ aim,
        const u16* __restrict__ cre, const u16* __restrict__ cim, const u16* __restrict__ ldt, const u16* __restrict__ dsk,
        u16* __restrict__ out, unsigned char* ws) {
    extern __shared__ __attribute__((aligned(16))) unsigned char smem[];
    LAS unsigned char* L = (LAS unsigned char*)smem;
    const int t = threadIdx.x, l = t & 63;
    const int w = __builtin_amdgcn_readfirstlane(t >> 6);
    const int wg = (blockIdx.x & 7) * 32 + (blockIdx.x >> 3);
    const int c1 = w >> 2, m = t & 255;
    const int th = (w >> 1) & 1, rh = w & 1;
    const int n16 = l & 15, g = l >> 4;
    const __amdgpu_buffer_rsrc_t box = __builtin_amdgcn_make_buffer_rsrc(ws + WS_BOX, 0, (int)(5 * MB2), 0x00020000);
    STAMP(0);

    const int chg = 2 * wg + c1;
    const v4u kv16 = *(const v4u*)((const u16*)(ws + WS_KVT) + (size_t)chg * SEQ + 8 * m);
    const v4u fcur = *(const v4u*)(ssmk + (size_t)chg * SEQ + 8 * m);
    v4u fprv = *(const v4u*)(ssmk + (size_t)chg * SEQ + 8 * (m > 0 ? m - 1 : 0));
    const int row = 8 * wg + w;
    const v4u q16 = *(const v4u*)(qin + (size_t)row * DM + 8 * l);
    const v4u kd16 = *(const v4u*)((const u16*)(ws + WS_KVD) + (size_t)row * DM + 8 * l);
    const v4u ds16 = *(const v4u*)(dsk + 8 * l);
    const int pch = 2 * wg + ((t >> 7) & 1), pn = t & 127;
    const u32 r_lar = lar[pch * NS + pn], r_aim = aim[pch * NS + pn], r_cre = cre[pch * NS + pn], r_cim = cim[pch * NS + pn], r_ldt = ldt[pch];

    if (m < 30) *(LAS v4u*)(L + L_U + c1 * U_S + 16 * m) = (v4u){0u, 0u, 0u, 0u};
    if (m < 8) *(LAS v4u*)(L + L_KC + (c1 * 8 + m) * KC_S) = (v4u){0u, 0u, 0u, 0u};
    if (m == 0) fprv = (v4u){0u, 0u, 0u, 0u};
    *(LAS v4u*)(L + L_U + c1 * U_S + 480 + 16 * m) = kv16;
    write_copies(L + L_KC + c1 * 8 * KC_S + 16 * (m + 1), fprv, fcur);
    {
        const v4u s = {SENT, SENT, SENT, SENT};
#pragma unroll
        for (int e = 1; e < 5; ++e) __builtin_amdgcn_raw_buffer_store_b128(s, box, (u32)(e * MB2) + wg * 8192 + t * 16, 0, 16);
    }
    __syncthreads();
    STAMP(1);

    LAS const unsigned char* abase = L + L_KC + c1 * 8 * KC_S + (n16 & 7) * KC_S + 16 * (3 + (n16 >> 3) - g) + 256 * rh;
    LAS const unsigned char* bbase = L + L_U + c1 * U_S + 256 + 32 * n16 + 16 * g - 256 * rh;
    LAS unsigned char* pbase = L + L_P + (rh * 2 + c1) * 8192 + (g * 16 + ((n16 + 4 * g) & 15)) * 16;
    const int eg = l & 3, en = l >> 2;
    LAS const unsigned char* prd = L + L_P + w * 1024 + (eg * 16 + ((en + 4 * eg) & 15)) * 16;
    const u32 pub_col = (u32)((w * 16 + en) * 16384 + wg * 64 + eg * 16);
    const u32 get_row = (u32)((wg >> 1) * 16384 + (t >> 1) * 64 + (wg & 1) * 32 + (t & 1) * 16);
    const u32 pub_row = (u32)((t >> 2) * 16384 + wg * 64 + (t & 3) * 16);

    v4u af[4][8];
    load_afrags(af, abase);
    if (th) conv_core<true, 1>(af, bbase, pbase); else conv_core<true, 0>(af, bbase, pbase);
    asm volatile("s_waitcnt vmcnt(0)" ::: "memory");
    __syncthreads();
    STAMP(2);
    {
        const f4 a0 = *(LAS const f4*)(prd), a1 = *(LAS const f4*)(prd + 8192), b0 = *(LAS const f4*)(prd + 16384), b1 = *(LAS const f4*)(prd + 24576);
        v4u o;
#pragma unroll
        for (int r = 0; r < 4; ++r) o[r] = clampS(pk_bf(a0[r] + b0[r], a1[r] + b1[r]));
        __builtin_amdgcn_raw_buffer_store_b128(o, box, pub_col, 0, 16);
    }
    STAMP(3);

    if (t < 256) {
        const float dt = rb(expf(bf2f(r_ldt)));
        const float are = rb(expf(bf2f(r_lar)));
        const float zr2 = -(are * dt) * 1.44269504088896340736f;
        const float zi = bf2f(r_aim) * dt;
        const float chi = 0.15915494309189533577f, clo = 6.420638326565253e-09f;
        const float fh = zi * chi;
        const float fl = fmaf(zi, chi, -fh) + zi * clo;
        const float fr = fh - __builtin_rintf(fh);
        const float F = fr + fl;
        const float F1 = __builtin_bit_cast(float, __builtin_bit_cast(u32, F) & 0xFFFFF000u);
        const float F2 = (fr - F1) + fl;
        const float cr = bf2f(r_cre), ci = bf2f(r_cim);
        const float mx = fmaxf(fabsf(cr), fabsf(ci)), mn = fminf(fabsf(cr), fabsf(ci));
        const float rt = mx > 0.f ? mn / mx : 0.f;
        const float lc = __log2f(mx) + 0.5f * __log2f(fmaf(rt, rt, 1.0f)) + __log2f(dt);
        const float ph = atan2f(ci, cr) * chi;
        *(LAS f4*)(L + L_PA + t * 16) = (f4){zr2, F1, F2, lc};
        *(LAS float*)(L + L_PH + t * 4) = ph;
        const float wm = wave_max(lc);
        if (l == 0) *(LAS float*)(L + L_MX + w * 4) = wm;
    }
    float xq[8], lnq[8], kd[8], dk[8];
    unpack8(q16, xq); unpack8(kd16, kd); unpack8(ds16, dk);
    ln_row(xq, lnq);
    __syncthreads();
    float ec0, ec1;
    {
        const f4 mxs = *(LAS const f4*)(L + L_MX);
        ec0 = fmaxf(__builtin_ceilf(fmaxf(mxs[0], mxs[1])), -120.f);
        ec1 = fmaxf(__builtin_ceilf(fmaxf(mxs[2], mxs[3])), -120.f);
        const float ec = c1 ? ec1 : ec0;
        const int r = n16;
        const float pB = (float)(8 * r);
        float pA[3];
        pA[0] = (float)((r & 7) + 128 * (r >> 3));
        pA[1] = (float)(r < 8 ? 256 + 128 * (r >> 2) + 2 * (r & 3) : 512 + 128 * ((r - 8) >> 1) + 4 * (r & 1));
        pA[2] = (float)(r < 8 ? 1024 + 128 * r : 2048);
        f4 dacc[3] = {{0.f, 0.f, 0.f, 0.f}, {0.f, 0.f, 0.f, 0.f}, {0.f, 0.f, 0.f, 0.f}};
#pragma unroll
        for (int k2 = 0; k2 < 2; ++k2) {
            const int nb = c1 * 128 + ((w & 3) * 2 + k2) * 16 + 4 * g;
            const f4 ph4 = *(LAS const f4*)(L + L_PH + nb * 4);
            v4u fb, fa[3];
#pragma unroll
            for (int i = 0; i < 4; ++i) {
                const f4 pp = *(LAS const f4*)(L + L_PA + (nb + i) * 16);
                const float lcn = pp[3] + (13.0f - ec);
                {
                    const float mag = __builtin_amdgcn_exp2f(fmaf(pp[0], pB, 14.0f));
                    const float an = __builtin_amdgcn_fractf(pp[1] * pB) + pp[2] * pB;
                    fb[i] = pk_h(mag * __builtin_amdgcn_cosf(an), mag * __builtin_amdgcn_sinf(an));
                }
#pragma unroll
                for (int mt = 0; mt < 3; ++mt) {
                    const float mag = __builtin_amdgcn_exp2f(fmaf(pp[0], pA[mt], lcn));
                    const float an = __builtin_amdgcn_fractf(pp[1] * pA[mt]) + fmaf(pp[2], pA[mt], ph4[i]);
                    fa[mt][i] = pk_h(mag * __builtin_amdgcn_cosf(an), -mag * __builtin_amdgcn_sinf(an));
                }
            }
#pragma unroll
            for (int mt = 0; mt < 3; ++mt) dacc[mt] = mm<false>(fa[mt], fb, dacc[mt]);
        }
#pragma unroll
        for (int mt = 0; mt < 3; ++mt) *(LAS f4*)(L + L_P + ((w * 3 + mt) * 64 + l) * 16) = dacc[mt];
    }
    STAMP(4);

    float yA[8], x[8], uD[8];
    {
        const v4u pc = box_get(ws + WS_BOX, 0 * (u32)MB2 + get_row);
        STAMP(5);
        LAS u32* ti = (LAS u32*)(L + L_TI) + (4 * (t & 1)) * 260 + (t >> 1);
        ti[0] = pc.x; ti[260] = pc.y; ti[520] = pc.z; ti[780] = pc.w;
        __syncthreads();
        float yc[8], u[8];
        unpack8(*(LAS const v4u*)(L + L_TI + w * 1040 + l * 16), yc);
#pragma unroll
        for (int i = 0; i < 8; ++i) { yA[i] = rb(yc[i] + kd[i]); x[i] = rb(xq[i] + rb(lnq[i] * yA[i])); }
        ln_row(x, u);
        *(LAS v4u*)(L + L_TO + w * 1088 + l * 16) = (v4u){pk_h(u[0] * USCALE, u[1] * USCALE), pk_h(u[2] * USCALE, u[3] * USCALE),
                                                         pk_h(u[4] * USCALE, u[5] * USCALE), pk_h(u[6] * USCALE, u[7] * USCALE)};
        __syncthreads();
        LAS const unsigned char* tr = L + L_TO + ((l & 15) >> 2) * 1088 + (w * 64 + (l >> 4) * 16 + (l & 3) * 4) * 2;
        const s4 lo = __builtin_amdgcn_ds_read_tr16_b64_v4i16((LAS s4*)tr);
        const s4 hi = __builtin_amdgcn_ds_read_tr16_b64_v4i16((LAS s4*)(tr + 4 * 1088));
        const v2u lo2 = __builtin_bit_cast(v2u, lo), hi2 = __builtin_bit_cast(v2u, hi);
        __builtin_amdgcn_raw_buffer_store_b128((v4u){clampS(lo2.x), clampS(lo2.y), clampS(hi2.x), clampS(hi2.y)}, box, 1 * (u32)MB2 + pub_row, 0, 16);
#pragma unroll
        for (int i = 0; i < 8; ++i) uD[i] = rb(u[i] * dk[i]);
    }
    STAMP(6);

    if (t < 384) {
        const int ch = t >= 192, rem = t - ch * 192, mt = rem >> 6, ll = rem & 63;
        LAS const unsigned char* pr = L + L_P + (((ch * 4) * 3 + mt) * 64 + ll) * 16;
        const f4 s = (*(LAS const f4*)(pr) + *(LAS const f4*)(pr + 3072)) + (*(LAS const f4*)(pr + 6144) + *(LAS const f4*)(pr + 9216));
        const int be = ll & 15;
#pragma unroll
        for (int i = 0; i < 4; ++i) {
            const int r = (ll >> 4) * 4 + i;
            int al;
            if (mt == 0) al = (r & 7) + 128 * (r >> 3);
            else if (mt == 1) al = r < 8 ? 256 + 128 * (r >> 2) + 2 * (r & 3) : 512 + 128 * ((r - 8) >> 1) + 4 * (r & 1);
            else al = 1024 + 128 * r;
            const int p = al + 8 * be;
            const int pidx = p < 128 ? p : (int)(__builtin_bit_cast(u32, (float)p) >> 16) - 0x4300 + 128;
            if (p <= 2048) *(LAS _Float16*)(L + L_KCMP + ch * 1312 + pidx * 2) = (_Float16)(s[i] * 1.9073486328125e-06f);
        }
    }
    __syncthreads();
    {
        u32 d[8];
#pragma unroll
        for (int i = 0; i < 8; ++i) {
            u32 v2[2];
#pragma unroll
            for (int h = 0; h < 2; ++h) {
                const int lag = 8 * m - 8 + 2 * i + h;
                const int lg = lag < 0 ? 0 : lag;
                const int pidx = lg < 128 ? lg : (int)(__builtin_bit_cast(u32, rb((float)lg)) >> 16) - 0x4300 + 128;
                const u32 v = *(LAS const u16*)(L + L_KCMP + c1 * 1312 + pidx * 2);
                v2[h] = lag < 0 ? 0u : v;
            }
            d[i] = v2[0] | (v2[1] << 16);
        }
        write_copies(L + L_KC + c1 * 8 * KC_S + 16 * (m + 1), (v4u){d[0], d[1], d[2], d[3]}, (v4u){d[4], d[5], d[6], d[7]});
    }
    __syncthreads();
    load_afrags(af, abase);
    const float sc0 = __builtin_amdgcn_exp2f(ec0 - 7.0f) * (1.0f / USCALE), sc1 = __builtin_amdgcn_exp2f(ec1 - 7.0f) * (1.0f / USCALE);
    STAMP(7);

#pragma unroll
    for (int it = 0; it < 2; ++it) {
        {
            const v4u pc = box_get(ws + WS_BOX, (u32)((1 + 2 * it) * MB2) + get_row);
            STAMP(8 + 6 * it);
            *(LAS v4u*)(L + L_U + (t & 1) * U_S + 480 + (t >> 1) * 16) = pc;
            __syncthreads();
            if (th) conv_core<false, 1>(af, bbase, pbase); else conv_core<false, 0>(af, bbase, pbase);
            __syncthreads();
            STAMP(9 + 6 * it);
            const f4 a0 = *(LAS const f4*)(prd), a1 = *(LAS const f4*)(prd + 8192), b0 = *(LAS const f4*)(prd + 16384), b1 = *(LAS const f4*)(prd + 24576);
            v4u o;
#pragma unroll
            for (int r = 0; r < 4; ++r) o[r] = clampS(pk_bf((a0[r] + b0[r]) * sc0, (a1[r] + b1[r]) * sc1));
            __builtin_amdgcn_raw_buffer_store_b128(o, box, (u32)((2 + 2 * it) * MB2) + pub_col, 0, 16);
            STAMP(10 + 6 * it);
        }
        {
            const v4u pc = box_get(ws + WS_BOX, (u32)((2 + 2 * it) * MB2) + get_row);
            STAMP(11 + 6 * it);
            LAS u32* ti = (LAS u32*)(L + L_TI) + (4 * (t & 1)) * 260 + (t >> 1);
            ti[0] = pc.x; ti[260] = pc.y; ti[520] = pc.z; ti[780] = pc.w;
            __syncthreads();
            float yc[8], u[8];
            unpack8(*(LAS const v4u*)(L + L_TI + w * 1040 + l * 16), yc);
            if (it == 0) {
#pragma unroll
                for (int i = 0; i < 8; ++i) x[i] = rb(x[i] + rb(yc[i] + uD[i]));
                ln_row(x, u);
#pragma unroll
                for (int i = 0; i < 8; ++i) x[i] = rb(x[i] + rb(u[i] * yA[i]));
                ln_row(x, u);
                *(LAS v4u*)(L + L_TO + w * 1088 + l * 16) = (v4u){pk_h(u[0] * USCALE, u[1] * USCALE), pk_h(u[2] * USCALE, u[3] * USCALE),
                                                                 pk_h(u[4] * USCALE, u[5] * USCALE), pk_h(u[6] * USCALE, u[7] * USCALE)};
                __syncthreads();
                LAS const unsigned char* tr = L + L_TO + ((l & 15) >> 2) * 1088 + (w * 64 + (l >> 4) * 16 + (l & 3) * 4) * 2;
                const s4 lo = __builtin_amdgcn_ds_read_tr16_b64_v4i16((LAS s4*)tr);
                const s4 hi = __builtin_amdgcn_ds_read_tr16_b64_v4i16((LAS s4*)(tr + 4 * 1088));
                const v2u lo2 = __builtin_bit_cast(v2u, lo), hi2 = __builtin_bit_cast(v2u, hi);
                __builtin_amdgcn_raw_buffer_store_b128((v4u){clampS(lo2.x), clampS(lo2.y), clampS(hi2.x), clampS(hi2.y)}, box, 3 * (u32)MB2 + pub_row, 0, 16);
#pragma unroll
                for (int i = 0; i < 8; ++i) uD[i] = rb(u[i] * dk[i]);
            } else {
                float o[8];
#pragma unroll
                for (int i = 0; i < 8; ++i) o[i] = x[i] + rb(yc[i] + uD[i]);
                *(v4u*)(out + (size_t)row * DM + 8 * l) = (v4u){pk_bf(o[0], o[1]), pk_bf(o[2], o[3]), pk_bf(o[4], o[5]), pk_bf(o[6], o[7])};
            }
            STAMP(12 + 6 * it);
        }
    }
}

extern "C" void kernel_launch(void* const* d_in, const int* in_sizes, int n_in, void* d_out, int out_size, void* d_ws, size_t ws_size,
                              hipStream_t stream) {
    const u16* q = (const u16*)d_in[0];
    const u16* k = (const u16*)d_in[1];
    const u16* v = (const u16*)d_in[2];
    const u16* shk = (const u16*)d_in[3];
    const u16* ssmk = (const u16*)d_in[4];
    const u16* ssmD = (const u16*)d_in[5];
    const u16* lar = (const u16*)d_in[7];
    const u16* aim = (const u16*)d_in[8];
    const u16* cre = (const u16*)d_in[9];
    const u16* cim = (const u16*)d_in[10];
    const u16* ldt = (const u16*)d_in[11];
    const u16* dsk = (const u16*)d_in[12];
    unsigned char* ws = (unsigned char*)d_ws;
    (void)hipFuncSetAttribute((const void*)k1_main, hipFuncAttributeMaxDynamicSharedMemorySize, L_END);
    k0_prep<<<256, 256, 0, stream>>>(k, v, shk, ssmD, ws);
    k1_main<<<256, 512, L_END, stream>>>(q, ssmk, lar, aim, cre, cim, ldt, dsk, (u16*)d_out, ws);
}
```

```cpp
#include <hip/hip_runtime.h>

typedef unsigned short u16;
typedef unsigned u32;
typedef u32 v4u __attribute__((ext_vector_type(4)));
typedef u32 v2u __attribute__((ext_vector_type(2)));
typedef float f4 __attribute__((ext_vector_type(4)));
typedef float f2 __attribute__((ext_vector_type(2)));
typedef __bf16 bf8 __attribute__((ext_vector_type(8)));
typedef __bf16 bf2 __attribute__((ext_vector_type(2)));
typedef _Float16 h8 __attribute__((ext_vector_type(8)));
typedef _Float16 h2 __attribute__((ext_vector_type(2)));
typedef short s4 __attribute__((ext_vector_type(4)));
#define LAS __attribute__((address_space(3)))
#define DI __device__ __forceinline__
extern "C" __device__ unsigned long long amd_dispatch_id() __asm("llvm.amdgcn.dispatch.id");

constexpr int SEQ = 2048, DM = 512, NS = 128;
constexpr u32 SENT = 0xFFFFFFFFu;
constexpr u32 MB2 = 2u * 1024u * 1024u;
constexpr u32 WS_FLAG = 6 * MB2;
constexpr size_t WS_STAMP = 8 * (size_t)MB2;
constexpr float USCALE = 16.0f;
#define D_FULL 1150
#define D_HALF 800
#define D_X1 3300
#define D_X2 3700

constexpr int KC_S = 4160;
constexpr int L_KC = 0;
constexpr int U_S = 4672;
constexpr int L_U = L_KC + 2 * 4 * KC_S;
constexpr int L_P = L_U + 2 * U_S;
constexpr int L_TI = L_P + 32768;
constexpr int L_TO = L_TI + 8 * 1040;
constexpr int KCMP_S = 2832;
constexpr int L_KCMP = L_TO + 8 * 1088;
constexpr int TB_R = 544, TB_S = 57 * TB_R;
constexpr int L_TB = L_KCMP + 2 * KCMP_S;
constexpr int L_END = L_TB + 2 * TB_S;

DI float bf2f(u32 h) { return __builtin_bit_cast(float, h << 16); }
DI float lo2f(u32 d) { return __builtin_bit_cast(float, d << 16); }
DI float hi2f(u32 d) { return __builtin_bit_cast(float, d & 0xFFFF0000u); }
DI f2 up2(u32 d) { return (f2){lo2f(d), hi2f(d)}; }
DI float rb(float x) { f2 t = {0.f, x}; return __builtin_bit_cast(float, __builtin_convertvector(t, bf2)); }
DI u32 pk_bf(float a, float b) { f2 t = {a, b}; return __builtin_bit_cast(u32, __builtin_convertvector(t, bf2)); }
DI u32 pk_h(float a, float b) { f2 t = {a, b}; return __builtin_bit_cast(u32, __builtin_convertvector(t, h2)); }
DI h2 as_h2(u32 d) { return __builtin_bit_cast(h2, d); }
DI u32 clampS(u32 d) { return d < 0xFFFFFFFEu ? d : 0xFFFFFFFEu; }
DI f2 fma2(f2 a, f2 b, f2 c) { return __builtin_elementwise_fma(a, b, c); }
DI f2 sp2(float a) { return (f2){a, a}; }
template <int CTRL, int RM = 0xf> DI float dppf(float v) {
    return __builtin_bit_cast(float, __builtin_amdgcn_update_dpp(0, __builtin_bit_cast(int, v), CTRL, RM, 0xf, true));
}
DI float rdl(float v, int l) { return __builtin_bit_cast(float, __builtin_amdgcn_readlane(__builtin_bit_cast(int, v), l)); }
DI void wave_sum2(float& a, float& b) {
    const auto sw = __builtin_amdgcn_permlane32_swap(__builtin_bit_cast(u32, a), __builtin_bit_cast(u32, b), false, false);
    float r = __builtin_bit_cast(float, (u32)sw[0]) + __builtin_bit_cast(float, (u32)sw[1]);
    r += dppf<0xB1>(r); r += dppf<0x4E>(r); r += dppf<0x141>(r); r += dppf<0x140>(r);
    asm volatile("s_nop 1\n\tv_add_f32_dpp %0, %0, %0 row_bcast:15 row_mask:0xa bank_mask:0xf\n\ts_nop 0" : "+v"(r));
    a = rdl(r, 31); b = rdl(r, 63);
}
DI float wave_max(float v) {
    v = fmaxf(v, dppf<0xB1>(v)); v = fmaxf(v, dppf<0x4E>(v)); v = fmaxf(v, dppf<0x141>(v)); v = fmaxf(v, dppf<0x140>(v));
    v = fmaxf(v, dppf<0x142, 0xa>(v)); v = fmaxf(v, dppf<0x143, 0xc>(v));
    return rdl(v, 63);
}
DI void ln_stats(const f2* x, float scale, float& rs, float& nb) {
    const float c = rdl(x[0].x, 0);
    f2 d[4];
#pragma unroll
    for (int i = 0; i < 4; ++i) d[i] = x[i] - sp2(c);
    const f2 s2 = (d[0] + d[1]) + (d[2] + d[3]);
    const f2 q2 = fma2(d[3], d[3], fma2(d[2], d[2], fma2(d[1], d[1], d[0] * d[0])));
    float s = s2.x + s2.y, q = q2.x + q2.y;
    wave_sum2(s, q);
    const float md = s * (1.0f / DM);
    const float var = fmaxf(fmaf(-md, md, q * (1.0f / DM)), 0.f);
    const float mb = rb(c + md);
    const float sd = rb(__builtin_amdgcn_sqrtf(rb(rb(var) + 1e-5f)));
    rs = __builtin_amdgcn_rcpf(sd) * scale;
    nb = -mb * rs;
}

DI bool has_sent(v4u v) { const u32 a = v.x > v.y ? v.x : v.y, b = v.z > v.w ? v.z : v.w; return (a > b ? a : b) == SENT; }
DI v4u ld4_dev(const unsigned char* p) {
    v4u v; asm volatile("global_load_dwordx4 %0, %1, off sc1\n\ts_waitcnt vmcnt(0)" : "=v"(v) : "v"(p) : "memory"); return v;
}
DI v2u ld2_dev(const unsigned char* p) {
    v2u v; asm volatile("global_load_dwordx2 %0, %1, off sc1\n\ts_waitcnt vmcnt(0)" : "=v"(v) : "v"(p) : "memory"); return v;
}
#define SB() __builtin_amdgcn_sched_barrier(0)
#define FENCE() do { asm volatile("" ::: "memory"); SB(); } while (0)
DI u32 now() { unsigned long long t_; asm volatile("s_memtime %0\n\ts_waitcnt lgkmcnt(0)" : "=s"(t_) :: "memory"); return (u32)t_; }
DI void wait_until(u32 t0, u32 d) {
#pragma unroll 1
    for (int i = 0; i < 256; ++i) if (now() - t0 >= d) break;
}
DI v4u box_req(__amdgpu_buffer_rsrc_t rs, u32 off) {
    FENCE();
    const v4u v = __builtin_amdgcn_raw_buffer_load_b128(rs, off, 0, 16);
    FENCE();
    return v;
}
DI v4u box_fin(v4u v, const unsigned char* base, u32 off) {
#pragma unroll 1
    for (u32 sp = 0; sp < (1u << 16); ++sp) {
        const bool bad = has_sent(v);
        if (!__any(bad)) break;
        __builtin_amdgcn_s_sleep(2);
        if (bad) v = ld4_dev(base + off);
    }
    return v;
}
DI void lds_bar() { asm volatile("s_waitcnt lgkmcnt(0)\n\ts_barrier" ::: "memory"); }
DI void vm_wait() { asm volatile("s_waitcnt vmcnt(0)" ::: "memory"); }

DI void write_copies(LAS unsigned char* kc  , const u32* d) {
    u32 o[5];
#pragma unroll
    for (int i = 0; i < 5; ++i) o[i] = __builtin_amdgcn_alignbit(d[i + 1], d[i], 16);
    *(LAS v4u*)(kc) = (v4u){d[2], d[3], d[4], d[5]};
    *(LAS v4u*)(kc + KC_S) = (v4u){o[1], o[2], o[3], o[4]};
    *(LAS v4u*)(kc + 2 * KC_S) = (v4u){d[1], d[2], d[3], d[4]};
    *(LAS v4u*)(kc + 3 * KC_S) = (v4u){o[0], o[1], o[2], o[3]};
}

template <bool BF> DI f4 mm(v4u a, v4u b, f4 c) {
    if constexpr (BF) return __builtin_amdgcn_mfma_f32_16x16x32_bf16(__builtin_bit_cast(bf8, a), __builtin_bit_cast(bf8, b), c, 0, 0, 0);
    else return __builtin_amdgcn_mfma_f32_16x16x32_f16(__builtin_bit_cast(h8, a), __builtin_bit_cast(h8, b), c, 0, 0, 0);
}
DI v2u tr_read(LAS const unsigned char* p) { return __builtin_bit_cast(v2u, __builtin_amdgcn_ds_read_tr16_b64_v4i16((LAS s4*)p)); }

DI void load_afrags(v4u (&a)[4][8], LAS const unsigned char* abase) {
#pragma unroll
    for (int p = 0; p < 8; ++p)
#pragma unroll
        for (int ri = 0; ri < 4; ++ri) {
            const v2u lo = tr_read(abase + 512 * p + 64 * ri + 8), hi = tr_read(abase + 512 * p + 64 * ri);
            a[ri][p] = (v4u){lo.x, lo.y, hi.x, hi.y};
        }
}
template <int Q0, int QN> DI void load_b(v4u (&bq)[4][4], LAS const unsigned char* bbase) {
#pragma unroll
    for (int ri = 0; ri < 4; ++ri)
#pragma unroll
        for (int q = 0; q < QN; ++q) bq[ri][q] = *(LAS const v4u*)(bbase + 512 * (Q0 + q) + 64 * (3 - ri));
}
template <bool BF, int TH, int Q0, int TI0, int R0, int R1>
DI void conv_mm(const v4u (&a)[4][8], const v4u (&bq)[4][4], f4 (&acc)[4]) {
    constexpr int TL[4] = {TH ? 1 : 0, TH ? 2 : 3, TH ? 5 : 4, TH ? 6 : 7};
#pragma unroll
    for (int ri = R0; ri < R1; ++ri)
#pragma unroll
        for (int q = 0; q < 4; ++q)
#pragma unroll
            for (int ti = TI0; ti < TI0 + 2; ++ti)
                if (TL[ti] - Q0 - q >= 0) acc[ti] = mm<BF>(a[ri][TL[ti] - Q0 - q], bq[ri][q], acc[ti]);
}
template <int TH, int TI0> DI void put_part(const f4 (&acc)[4], LAS unsigned char* pbase) {
    constexpr int TL[4] = {TH ? 1 : 0, TH ? 2 : 3, TH ? 5 : 4, TH ? 6 : 7};
#pragma unroll
    for (int ti = TI0; ti < TI0 + 2; ++ti) *(LAS f4*)(pbase + TL[ti] * 1024) = acc[ti];
}

DI f2 cmul(f2 a, f2 b) {
    f2 t, r;
    asm("v_pk_mul_f32 %0, %1, %2 op_sel:[0,0] op_sel_hi:[0,1]" : "=v"(t) : "v"(a), "v"(b));
    asm("v_pk_fma_f32 %0, %1, %2, %3 op_sel:[1,1,0] op_sel_hi:[1,0,1] neg_lo:[0,1,0]" : "=v"(r) : "v"(a), "v"(b), "v"(t));
    return r;
}
DI f2 cexp_p(float zr2, float fh, float fl, float p) {
    const float mag = __builtin_amdgcn_exp2f(zr2 * p);
    const float an = fmaf(fl, p, __builtin_amdgcn_fractf(fh * p));
    return (f2){__builtin_amdgcn_cosf(an), __builtin_amdgcn_sinf(an)} * sp2(mag);
}
DI float exp_acc(float x) {
    const float L = 1.44269504088896340736f, Ll = 1.92596299112661746e-08f;
    const float h = x * L;
    const float lo = fmaf(x, Ll, fmaf(x, L, -h));
    const float e = __builtin_amdgcn_exp2f(h);
    return fmaf(e * lo, 0.69314718055994530942f, e);
}

#define STAMP(i) do {} while (0)

__global__ void __launch_bounds__(512, 2)
k1_main(const u16* __restrict__ qin, const u16* __restrict__ kin, const u16* __restrict__ vin, const u16* __restrict__ shk,
        const u16* __restrict__ ssmk, const u16* __restrict__ ssmD, const u16* __restrict__ lar, const u16* __restrict__ aim,
        const u16* __restrict__ cre, const u16* __restrict__ cim, const u16* __restrict__ ldt, const u16* __restrict__ dsk,
        u16* __restrict__ out, unsigned char* ws) {
    extern __shared__ __attribute__((aligned(16))) unsigned char smem[];
    LAS unsigned char* L = (LAS unsigned char*)smem;
    const int t = threadIdx.x, l = t & 63;
    const int w = __builtin_amdgcn_readfirstlane(t >> 6);
    const int wg = (blockIdx.x & 7) * 32 + (blockIdx.x >> 3);
    const int H = (wg >> 1) & 1;
    const int rbk = H * 128 + (wg >> 2) * 2 + (wg & 1);
    const int c1 = w >> 2, m = t & 255;
    const int th = (w >> 1) & 1, rh = w & 1;
    const int n16 = l & 15, g = l >> 4;
    const __amdgpu_buffer_rsrc_t box = __builtin_amdgcn_make_buffer_rsrc(ws, 0, (int)(WS_FLAG + 16384), 0x00020000);
    STAMP(0);
    const v4u sent4 = {SENT, SENT, SENT, SENT};
    const u32 pub_row = (u32)((t >> 2) * 16384 + rbk * 64 + (t & 3) * 16);
    const u32 ini_y = (u32)((rbk >> 1) * 16384 + w * 2048 + (rbk & 1) * 1024 + l * 16);
    const u32 ini_u = (u32)((wg >> 1) * 16384 + w * 2048 + (wg & 1) * 1024 + l * 16);
    __builtin_amdgcn_raw_buffer_store_b128(sent4, box, pub_row, 0, 16);
    __builtin_amdgcn_raw_buffer_store_b128(sent4, box, 1 * MB2 + ini_y, 0, 16);
    FENCE();

    const int cp = t >> 1, hh = t & 1;
    u32 kr[7], vr[4];
    {
        const __amdgpu_buffer_rsrc_t rk = __builtin_amdgcn_make_buffer_rsrc((void*)kin, 0, SEQ * DM * 2, 0x00020000);
        const __amdgpu_buffer_rsrc_t rv = __builtin_amdgcn_make_buffer_rsrc((void*)vin, 0, SEQ * DM * 2, 0x00020000);
        const int r0 = 8 * rbk + 4 * hh;
#pragma unroll
        for (int i = 0; i < 7; ++i) kr[i] = __builtin_amdgcn_raw_buffer_load_b32(rk, (r0 - 3 + i) * (DM * 2) + cp * 4, 0, 0);
#pragma unroll
        for (int i = 0; i < 4; ++i) vr[i] = __builtin_amdgcn_raw_buffer_load_b32(rv, (r0 + i) * (DM * 2) + cp * 4, 0, 0);
    }
    const v4u sw = *(const v4u*)(shk + cp * 8);
    FENCE();
    const int chg = 2 * wg + c1;
    const v4u fcur = *(const v4u*)(ssmk + (size_t)chg * SEQ + 8 * m);
    v2u fprv = *(const v2u*)(ssmk + (size_t)chg * SEQ + (m > 0 ? 8 * m - 4 : 0));
    const int pch = 2 * wg + ((t >> 7) & 1), pn = t & 127;
    const u32 r_lar = lar[pch * NS + pn], r_aim = aim[pch * NS + pn], r_cre = cre[pch * NS + pn], r_cim = cim[pch * NS + pn], r_ldt = ldt[pch];
    const u32 r_cre2 = cre[pch * NS + (pn ^ 64)], r_cim2 = cim[pch * NS + (pn ^ 64)];
    const int row = 8 * rbk + w;
    const v4u q16 = *(const v4u*)(qin + (size_t)row * DM + 8 * l);
    const u32 dA2 = *(const u32*)(ssmD + 2 * wg), dB2 = *(const u32*)(dsk + 2 * wg);
    __builtin_amdgcn_raw_buffer_store_b128(sent4, box, 2 * MB2 + ini_u, 0, 16);
    FENCE();
    v2u nonce;
    {
        const unsigned long long id = amd_dispatch_id(), dp = (unsigned long long)__builtin_amdgcn_dispatch_ptr();
        const u32 a = (u32)id * 0x9E3779B1u + (u32)(dp >> 32);
        const u32 b = ((u32)dp ^ ((u32)(id >> 32) * 0x85EBCA77u)) & ~1u;
        nonce = (v2u){a, b | (~a & 1u)};
    }

    if (m < 30) *(LAS v4u*)(L + L_U + c1 * U_S + 16 * m) = (v4u){0u, 0u, 0u, 0u};
    if (m < 8) *(LAS v4u*)(L + L_KC + (c1 * 4 + (m >> 1)) * KC_S + (m & 1) * 16) = (v4u){0u, 0u, 0u, 0u};

    LAS const unsigned char* abase = L + L_KC + (c1 * 4 + (n16 >> 2)) * KC_S + 56 - 16 * g + 8 * (n16 & 3) + 256 * rh;
    LAS const unsigned char* bbase = L + L_U + c1 * U_S + 256 + 32 * n16 + 16 * g - 256 * rh;
    LAS unsigned char* pbase = L + L_P + (rh * 2 + c1) * 8192 + (g * 16 + ((n16 + 4 * g) & 15)) * 16;
    const int eg = l & 3, en = l >> 2;
    LAS const unsigned char* prd = L + L_P + w * 1024 + (eg * 16 + ((en + 4 * eg) & 15)) * 16;
    LAS const unsigned char* urd = L + L_U + 480 + w * 512 + l * 8;
    LAS unsigned char* ust = L + L_U + (t & 1) * U_S + 480 + (t >> 1) * 16;
    const u32 pub_col = (u32)((w * 16 + en) * 16384 + wg * 64 + eg * 16);
    const u32 get_u = (u32)((wg >> 1) * 16384 + (t >> 1) * 64 + (wg & 1) * 32 + (t & 1) * 16);
    const u32 get_y = (u32)((rbk >> 1) * 16384 + (t >> 1) * 64 + (rbk & 1) * 32 + (t & 1) * 16);
    const u32 flag_w = WS_FLAG + (u32)(w * 256 + rbk) * 8, flag_r = WS_FLAG + (u32)((wg >> 5) * 256 + (t >> 1)) * 8;

    u32 t1;
    {
        asm volatile("" :: "v"(kr[0]) : "memory");
        if (l == 0) __builtin_amdgcn_raw_buffer_store_b64(nonce, box, flag_w, 0, 16);
        const f2 w0 = {lo2f(sw.x), lo2f(sw.z)}, w1 = {hi2f(sw.x), hi2f(sw.z)}, w2 = {lo2f(sw.y), lo2f(sw.w)}, w3 = {hi2f(sw.y), hi2f(sw.w)};
        f2 kf[7], kv[4];
#pragma unroll
        for (int i = 0; i < 7; ++i) kf[i] = up2(kr[i]);
#pragma unroll
        for (int r = 0; r < 4; ++r) kv[r] = fma2(kf[r + 3], w0, fma2(kf[r + 2], w1, fma2(kf[r + 1], w2, kf[r] * w3))) * up2(vr[r]);
        const u32 p00 = clampS(pk_bf(kv[0].x, kv[1].x)), p01 = clampS(pk_bf(kv[2].x, kv[3].x));
        const u32 p10 = clampS(pk_bf(kv[0].y, kv[1].y)), p11 = clampS(pk_bf(kv[2].y, kv[3].y));
        const u32 s0 = hh ? p00 : p10, s1 = hh ? p01 : p11;
        const u32 r0 = __builtin_amdgcn_update_dpp(0, s0, 0xB1, 0xf, 0xf, true), r1 = __builtin_amdgcn_update_dpp(0, s1, 0xB1, 0xf, 0xf, true);
        const v4u o = hh ? (v4u){r0, r1, p10, p11} : (v4u){p00, p01, r0, r1};
        __builtin_amdgcn_raw_buffer_store_b128(o, box, pub_row, 0, 16);
        FENCE();
        t1 = now();
    }
    STAMP(1);
    {
        if (m == 0) fprv = (v2u){0u, 0u};
        const u32 d[6] = {fprv.x, fprv.y, fcur.x, fcur.y, fcur.z, fcur.w};
        write_copies(L + L_KC + c1 * 4 * KC_S + 32 + 16 * m, d);
    }
    v2u fl = __builtin_amdgcn_raw_buffer_load_b64(box, flag_r, 0, 16);
    lds_bar();
    v4u af[4][8];
    load_afrags(af, abase);
    STAMP(2);
    {
        const float dt = rb(exp_acc(bf2f(r_ldt)));
        const float are = rb(exp_acc(bf2f(r_lar)));
        const float zr2 = -(are * dt) * 1.44269504088896340736f;
        const float zi = bf2f(r_aim) * dt;
        const float chi = 0.15915494309189533577f, clo = 6.420638326565253e-09f;
        const float fh = zi * chi;
        const float fl2 = fmaf(zi, clo, fmaf(zi, chi, -fh));
        const u32 am = max(max(r_cre & 0x7FFFu, r_cim & 0x7FFFu), max(r_cre2 & 0x7FFFu, r_cim2 & 0x7FFFu));
        const float mx = wave_max(bf2f(am)) * dt;
        int ec = __builtin_amdgcn_frexp_expf(mx);
        ec = ec < -100 ? -100 : ec;
        const float sA = __builtin_amdgcn_ldexpf(1.0f, 13 - ec);
        const f2 C = (f2){bf2f(r_cre), -bf2f(r_cim)} * sp2(dt) * sp2(sA);
        LAS unsigned char* tb = L + L_TB + ((t >> 7) & 1) * TB_S + pn * 4;
        const f2 e128 = cexp_p(zr2, -fh, -fl2, 128.f);
#define PUT(rowi, v) *(LAS u32*)(tb + (rowi) * TB_R) = pk_h((v).x, (v).y)
        if (w < 4) {
            const f2 e4 = cexp_p(zr2, -fh, -fl2, 4.f);
            f2 G = cmul(C, cexp_p(zr2, -fh, -fl2, 512.f));
            f2 e8 = cmul(e4, e4); e8.y = -e8.y;
            f2 B = {16384.f, 0.f};
#pragma unroll
            for (int h = 4; h <= 16; ++h) {
                if (h < 8) { PUT(24 + 2 * (h - 4), G); const f2 G4 = cmul(G, e4); PUT(25 + 2 * (h - 4), G4); }
                else PUT(24 + h, G);
                if (h < 16) G = cmul(G, e128);
            }
#pragma unroll
            for (int r = 0; r < 16; ++r) { PUT(41 + r, B); if (r < 15) B = cmul(B, e8); }
        } else {
            f2 e[8];
            e[1] = cexp_p(zr2, -fh, -fl2, 1.f);
            e[2] = cmul(e[1], e[1]); e[3] = cmul(e[2], e[1]); e[4] = cmul(e[2], e[2]);
            e[5] = cmul(e[4], e[1]); e[6] = cmul(e[4], e[2]); e[7] = cmul(e[4], e[3]);
            f2 G = C;
#pragma unroll
            for (int h = 0; h < 4; ++h) {
                if (h < 2) {
                    PUT(8 * h, G);
#pragma unroll
                    for (int a = 1; a < 8; ++a) { const f2 v = cmul(G, e[a]); PUT(8 * h + a, v); }
                } else {
                    PUT(16 + 4 * (h - 2), G);
#pragma unroll
                    for (int a = 1; a < 4; ++a) { const f2 v = cmul(G, e[2 * a]); PUT(16 + 4 * (h - 2) + a, v); }
                }
                if (h < 3) G = cmul(G, e128);
            }
        }
#undef PUT
        if (l == 0) *(LAS int*)(L + L_KCMP + ((t >> 7) & 1) * KCMP_S + 2824) = ec;
    }
    STAMP(3);
#pragma unroll 1
    for (u32 sp = 0; sp < (1u << 16); ++sp) {
        const bool bad = fl.x != nonce.x || fl.y != nonce.y;
        if (!__any(bad)) break;
        __builtin_amdgcn_s_sleep(2);
        if (bad) fl = ld2_dev(ws + flag_r);
    }
    wait_until(t1, D_FULL);
    v4u pc = box_req(box, get_u);
    STAMP(4);
    float rs0, nb0;
    {
        const f2 xq[4] = {up2(q16.x), up2(q16.y), up2(q16.z), up2(q16.w)};
        ln_stats(xq, 1.0f, rs0, nb0);
    }
    lds_bar();
    if (w < 6) {
        const int ch = w >= 3, mt = w - 3 * ch;
        LAS const unsigned char* ta = L + L_TB + ch * TB_S + (16 * mt + n16) * TB_R + 16 * g;
        LAS const unsigned char* tbb = L + L_TB + ch * TB_S + (41 + n16) * TB_R + 16 * g;
        f4 dacc = {0.f, 0.f, 0.f, 0.f};
#pragma unroll
        for (int s = 0; s < 8; ++s) dacc = mm<false>(*(LAS const v4u*)(ta + 64 * s), *(LAS const v4u*)(tbb + 64 * s), dacc);
        const int pb = 8 * n16 + (mt == 0 ? 4 * (g & 1) + 128 * (g >> 1) : mt == 1 ? (g < 2 ? 256 + 128 * g : 256 * g) : 1024 + 512 * g);
        const int s1 = mt == 0 ? 1 : mt == 1 ? (g < 2 ? 2 : 4) : 128, s2 = mt == 0 ? 2 : mt == 1 ? (g < 2 ? 4 : 128) : 256;
#pragma unroll
        for (int i = 0; i < 4; ++i) {
            int p = pb + (i & 1) * s1 + (i >> 1) * s2;
            p = p > 2048 ? 2064 : p;
            u32 ad = __builtin_bit_cast(u32, (float)p) >> 15;
            if (i == 0) ad = ad > 0x7EFEu ? ad : 0x7EFEu;
            *(LAS _Float16*)(L + L_KCMP + ch * KCMP_S - 0x7EFE + ad) = (_Float16)(dacc[i] * 1.9073486328125e-06f);
        }
    }
    STAMP(5);

    f4 acc[4];
    v4u bq[4][4];
    v4u py;
    u32 t2;
    {
        pc = box_fin(pc, ws, get_u);
        STAMP(6);
        *(LAS v4u*)ust = pc;
        __builtin_amdgcn_raw_buffer_store_b128(sent4, box, 3 * MB2 + ini_y, 0, 16);
        __builtin_amdgcn_raw_buffer_store_b128(sent4, box, 4 * MB2 + ini_u, 0, 16);
        lds_bar();
        STAMP(7);
        load_b<0, 4>(bq, bbase);
#pragma unroll
        for (int i = 0; i < 4; ++i) acc[i] = (f4){0.f, 0.f, 0.f, 0.f};
        SB();
        if (th) { conv_mm<true, 1, 0, 0, 0, 4>(af, bq, acc); conv_mm<true, 1, 0, 2, 0, 1>(af, bq, acc); put_part<1, 0>(acc, pbase); conv_mm<true, 1, 0, 2, 1, 2>(af, bq, acc); }
        else    { conv_mm<true, 0, 0, 0, 0, 4>(af, bq, acc); conv_mm<true, 0, 0, 2, 0, 1>(af, bq, acc); put_part<0, 0>(acc, pbase); conv_mm<true, 0, 0, 2, 1, 2>(af, bq, acc); }
        const f2 dd = up2(dA2);
#define SKIP_TERMS_A() \
        const v2u i0 = *(LAS const v2u*)(urd), i1 = *(LAS const v2u*)(urd + U_S); \
        const f2 e[4] = {(f2){lo2f(i0.x), lo2f(i1.x)} * dd, (f2){hi2f(i0.x), hi2f(i1.x)} * dd, (f2){lo2f(i0.y), lo2f(i1.y)} * dd, (f2){hi2f(i0.y), hi2f(i1.y)} * dd}
        lds_bar();
        STAMP(8);
#define EPILOGUE(BOXI, EXPR) do { \
            const f4 a0 = *(LAS const f4*)(prd), a1 = *(LAS const f4*)(prd + 8192), b0 = *(LAS const f4*)(prd + 16384), b1 = *(LAS const f4*)(prd + 24576); \
            const f4 y0 = a0 + b0, y1 = a1 + b1; \
            v4u o; \
            _Pragma("unroll") for (int r = 0; r < 4; ++r) { const f2 yy = EXPR; o[r] = clampS(pk_bf(yy.x, yy.y)); } \
            __builtin_amdgcn_raw_buffer_store_b128(o, box, (BOXI) * MB2 + pub_col, 0, 16); \
            FENCE(); } while (0)
        if (w < 4) { SKIP_TERMS_A(); EPILOGUE(1, ((f2){y0[r], y1[r]} + e[r])); }
        {
            u32 d[6];
#pragma unroll
            for (int i = 0; i < 6; ++i) {
                const int lag = 8 * m - 4 + 2 * i;
                const float fl0 = (float)(lag < 0 ? 0 : lag);
                const u32 cd = pk_bf(fl0, fl0 + 1.0f);
                const u32 c0 = (cd & 0xFFFFu) > 0x3F7Fu ? (cd & 0xFFFFu) : 0x3F7Fu, c1h = cd >> 16;
                LAS const unsigned char* kb = L + L_KCMP + c1 * KCMP_S - 2 * 0x3F7F;
                const u32 v0 = *(LAS const u16*)(kb + 2 * c0), v1 = *(LAS const u16*)(kb + 2 * c1h);
                d[i] = lag < 0 ? 0u : (v0 | (v1 << 16));
            }
            if (th) conv_mm<true, 1, 0, 2, 2, 4>(af, bq, acc); else conv_mm<true, 0, 0, 2, 2, 4>(af, bq, acc);
            write_copies(L + L_KC + c1 * 4 * KC_S + 32 + 16 * m, d);
        }
        SB();
        load_b<4, 4>(bq, bbase);
        SKIP_TERMS_A();
        SB();
        if (th) { conv_mm<true, 1, 4, 2, 0, 4>(af, bq, acc); put_part<1, 2>(acc, pbase); }
        else    { conv_mm<true, 0, 4, 2, 0, 4>(af, bq, acc); put_part<0, 2>(acc, pbase); }
        if (!H) py = box_req(box, 1 * MB2 + get_y);
        lds_bar();
        STAMP(9);
        if (w >= 4) EPILOGUE(1, ((f2){y0[r], y1[r]} + e[r]));
        t2 = now();
    }
    STAMP(10);
    const int ec0 = *(LAS const int*)(L + L_KCMP + 2824), ec1 = *(LAS const int*)(L + L_KCMP + KCMP_S + 2824);
    const f2 sc = {__builtin_amdgcn_ldexpf(1.0f / USCALE, ec0 - 7), __builtin_amdgcn_ldexpf(1.0f / USCALE, ec1 - 7)};
    if (H) load_afrags(af, abase);

    v4u yAp;
    f2 x[4] = {up2(q16.x), up2(q16.y), up2(q16.z), up2(q16.w)};
    LAS u32* ti = (LAS u32*)(L + L_TI) + (4 * (t & 1)) * 260 + (t >> 1);
    LAS const unsigned char* tr = L + L_TO + ((l & 15) >> 2) * 1088 + (w * 64 + (l >> 4) * 16 + (l & 3) * 4) * 2;
#pragma unroll
    for (int it = 0; it < 3; ++it) {
        u32 t3;
        {
            f2 gate[4];
            if (it == 0) {
#pragma unroll
                for (int i = 0; i < 4; ++i) gate[i] = fma2(x[i], sp2(rs0), sp2(nb0));
            } else if (it == 1) { gate[0] = up2(yAp.x); gate[1] = up2(yAp.y); gate[2] = up2(yAp.z); gate[3] = up2(yAp.w); }
            if (it < 2) asm volatile("" : "+v"(gate[0]), "+v"(gate[1]), "+v"(gate[2]), "+v"(gate[3]));
            if (H) { wait_until(t2, D_HALF); py = box_req(box, (1 + 2 * it) * MB2 + get_y); }
            py = box_fin(py, ws, (1 + 2 * it) * MB2 + get_y);
            STAMP(11 + 8 * it);
            ti[0] = py.x; ti[260] = py.y; ti[520] = py.z; ti[780] = py.w;
            if (it < 2 && H && w < 4) pc = box_req(box, (2 + 2 * it) * MB2 + get_u);
            lds_bar();
            STAMP(12 + 8 * it);
            const v4u yv = *(LAS const v4u*)(L + L_TI + w * 1040 + l * 16);
            const f2 yc[4] = {up2(yv.x), up2(yv.y), up2(yv.z), up2(yv.w)};
            float rs, nb;
            if (it == 0) {
#pragma unroll
                for (int i = 0; i < 4; ++i) x[i] = fma2(gate[i], yc[i], x[i]);
                yAp = yv;
            } else {
#pragma unroll
                for (int i = 0; i < 4; ++i) x[i] += yc[i];
            }
            if (it == 2) {
                *(v4u*)(out + (size_t)row * DM + 8 * l) = (v4u){pk_bf(x[0].x, x[0].y), pk_bf(x[1].x, x[1].y), pk_bf(x[2].x, x[2].y), pk_bf(x[3].x, x[3].y)};
                STAMP(13 + 8 * it);
                break;
            }
            if (it == 1) {
                ln_stats(x, 1.0f, rs, nb);
#pragma unroll
                for (int i = 0; i < 4; ++i) x[i] = fma2(fma2(x[i], sp2(rs), sp2(nb)), gate[i], x[i]);
            }
            ln_stats(x, USCALE, rs, nb);
            v4u uo;
#pragma unroll
            for (int i = 0; i < 4; ++i) { const f2 u = fma2(x[i], sp2(rs), sp2(nb)); uo[i] = pk_h(u.x, u.y); }
            *(LAS v4u*)(L + L_TO + w * 1088 + l * 16) = uo;
            lds_bar();
            const v2u lo2 = tr_read(tr), hi2 = tr_read(tr + 4 * 1088);
            __builtin_amdgcn_raw_buffer_store_b128((v4u){clampS(lo2.x), clampS(lo2.y), clampS(hi2.x), clampS(hi2.y)}, box, (2 + 2 * it) * MB2 + pub_row, 0, 16);
            FENCE();
            t3 = now();
            STAMP(13 + 8 * it);
        }
        if (it == 0) {
            if (!H) load_afrags(af, abase);
            if (l == 0) __builtin_amdgcn_raw_buffer_store_b64((v2u){~nonce.x, ~nonce.y}, box, flag_w, 0, 16);
        }
        {
            if (w < 4) {
                if (H) pc = box_fin(pc, ws, (2 + 2 * it) * MB2 + get_u);
                else { wait_until(t3, D_HALF); pc = box_req(box, (2 + 2 * it) * MB2 + get_u); pc = box_fin(pc, ws, (2 + 2 * it) * MB2 + get_u); }
                *(LAS v4u*)ust = pc;
            }
            STAMP(14 + 8 * it);
            lds_bar();
            STAMP(15 + 8 * it);
            load_b<0, 4>(bq, bbase);
#pragma unroll
            for (int i = 0; i < 4; ++i) acc[i] = (f4){0.f, 0.f, 0.f, 0.f};
            SB();
            if (th) { conv_mm<false, 1, 0, 0, 0, 4>(af, bq, acc); conv_mm<false, 1, 0, 2, 0, 1>(af, bq, acc); put_part<1, 0>(acc, pbase); conv_mm<false, 1, 0, 2, 1, 2>(af, bq, acc); }
            else    { conv_mm<false, 0, 0, 0, 0, 4>(af, bq, acc); conv_mm<false, 0, 0, 2, 0, 1>(af, bq, acc); put_part<0, 0>(acc, pbase); conv_mm<false, 0, 0, 2, 1, 2>(af, bq, acc); }
            const f2 dd = up2(dB2) * sp2(1.0f / USCALE);
#define SKIP_TERMS() \
            const v2u i0 = *(LAS const v2u*)(urd), i1 = *(LAS const v2u*)(urd + U_S); \
            const h2 h00 = as_h2(i0.x), h01 = as_h2(i0.y), h10 = as_h2(i1.x), h11 = as_h2(i1.y); \
            const f2 e[4] = {(f2){(float)h00.x, (float)h10.x} * dd, (f2){(float)h00.y, (float)h10.y} * dd, (f2){(float)h01.x, (float)h11.x} * dd, (f2){(float)h01.y, (float)h11.y} * dd}
            if (w >= 4 && H) { wait_until(t3, D_HALF); pc = box_req(box, (2 + 2 * it) * MB2 + get_u); }
            lds_bar();
            if (w < 4) { SKIP_TERMS(); EPILOGUE(3 + 2 * it, fma2((f2){y0[r], y1[r]}, sc, e[r])); }
            if (it == 0) __builtin_amdgcn_raw_buffer_store_b128(sent4, box, 5 * MB2 + ini_y, 0, 16);
            if (th) conv_mm<false, 1, 0, 2, 2, 4>(af, bq, acc); else conv_mm<false, 0, 0, 2, 2, 4>(af, bq, acc);
            STAMP(16 + 8 * it);
            if (w >= 4) {
                if (H) pc = box_fin(pc, ws, (2 + 2 * it) * MB2 + get_u);
                else { wait_until(t2, it ? D_X2 : D_X1); pc = box_req(box, (2 + 2 * it) * MB2 + get_u); pc = box_fin(pc, ws, (2 + 2 * it) * MB2 + get_u); }
                *(LAS v4u*)ust = pc;
            }
            lds_bar();
            STAMP(17 + 8 * it);
            load_b<4, 4>(bq, bbase);
            SKIP_TERMS();
            SB();
            if (th) { conv_mm<false, 1, 4, 2, 0, 4>(af, bq, acc); put_part<1, 2>(acc, pbase); }
            else    { conv_mm<false, 0, 4, 2, 0, 4>(af, bq, acc); put_part<0, 2>(acc, pbase); }
            if (!H) py = box_req(box, (3 + 2 * it) * MB2 + get_y);
            lds_bar();
            if (w >= 4) EPILOGUE(3 + 2 * it, fma2((f2){y0[r], y1[r]}, sc, e[r]));
            t2 = now();
            STAMP(18 + 8 * it);
        }
    }
}

extern "C" void kernel_launch(void* const* d_in, const int* in_sizes, int n_in, void* d_out, int out_size, void* d_ws, size_t ws_size,
                              hipStream_t stream) {
    (void)hipFuncSetAttribute((const void*)k1_main, hipFuncAttributeMaxDynamicSharedMemorySize, L_END);
    k1_main<<<256, 512, L_END, stream>>>((const u16*)d_in[0], (const u16*)d_in[1], (const u16*)d_in[2], (const u16*)d_in[3], (const u16*)d_in[4],
                                         (const u16*)d_in[5], (const u16*)d_in[7], (const u16*)d_in[8], (const u16*)d_in[9], (const u16*)d_in[10],
                                         (const u16*)d_in[11], (const u16*)d_in[12], (u16*)d_out, (unsigned char*)d_ws);
}
```

```cpp
#include <hip/hip_runtime.h>

typedef unsigned short u16;
typedef unsigned u32;
typedef u32 v4u __attribute__((ext_vector_type(4)));
typedef u32 v2u __attribute__((ext_vector_type(2)));
typedef float f4 __attribute__((ext_vector_type(4)));
typedef float f2 __attribute__((ext_vector_type(2)));
typedef __bf16 bf8 __attribute__((ext_vector_type(8)));
typedef __bf16 bf2 __attribute__((ext_vector_type(2)));
typedef _Float16 h8 __attribute__((ext_vector_type(8)));
typedef _Float16 h2 __attribute__((ext_vector_type(2)));
typedef short s4 __attribute__((ext_vector_type(4)));
typedef unsigned short us2 __attribute__((ext_vector_type(2)));
#define LAS __attribute__((address_space(3)))
#define DI __device__ __forceinline__
extern "C" __device__ unsigned long long amd_dispatch_id() __asm("llvm.amdgcn.dispatch.id");

constexpr int SEQ = 2048, DM = 512, NS = 128;
constexpr u32 SENT = 0xFFFFFFFFu;
constexpr u32 MB2 = 2u * 1024u * 1024u;
constexpr u32 WS_FLAG = 6 * MB2;
constexpr size_t WS_STAMP = 8 * (size_t)MB2;
constexpr float USCALE = 16.0f;
#define D_FL 700
#define MID_A 0
#define MID_B 8
#define MID_C 0
#define MID_D 2
#define FL_K 25
#define DLY 0
#define INI_EARLY 1
#define S_YA0 6500
#define S_YA1 9000
#define S_U00 11000
#define S_U10 13500
#define S_Y00 15500
#define S_Y10 18000
#define S_U01 20500
#define S_U11 23000
#define S_Y01 25000
#define S_Y11 27500

constexpr int KC_S = 4160;
constexpr int L_KC = 0;
constexpr int U_S = 4672;
constexpr int L_U = L_KC + 2 * 4 * KC_S;
constexpr int L_TI = L_U + 2 * U_S;
constexpr int L_TO = L_TI + 8 * 1040;
constexpr int KCMP_S = 2832;
constexpr int L_KCMP = L_TO + 8 * 1088;
constexpr int TB_R = 544, TB_S = 57 * TB_R;
constexpr int L_TB = L_KCMP + 2 * KCMP_S;
constexpr int L_P = L_TB + 2 * TB_S;
constexpr int KV_S = 1056;
constexpr int L_EP = L_P + 32768;
constexpr int L_END = L_EP + 64;

DI float bf2f(u32 h) { return __builtin_bit_cast(float, h << 16); }
DI float lo2f(u32 d) { return __builtin_bit_cast(float, d << 16); }
DI float hi2f(u32 d) { return __builtin_bit_cast(float, d & 0xFFFF0000u); }
DI f2 up2(u32 d) { return (f2){lo2f(d), hi2f(d)}; }
DI float rb(float x) { f2 t = {0.f, x}; return __builtin_bit_cast(float, __builtin_convertvector(t, bf2)); }
DI u32 pk_bf(float a, float b) { f2 t = {a, b}; return __builtin_bit_cast(u32, __builtin_convertvector(t, bf2)); }
DI u32 pk_h(float a, float b) { f2 t = {a, b}; return __builtin_bit_cast(u32, __builtin_convertvector(t, h2)); }
DI h2 as_h2(u32 d) { return __builtin_bit_cast(h2, d); }
DI u32 clampS(u32 d) { return d < 0xFFFFFFFEu ? d : 0xFFFFFFFEu; }
DI f2 fma2(f2 a, f2 b, f2 c) { return __builtin_elementwise_fma(a, b, c); }
DI f2 sp2(float a) { return (f2){a, a}; }
template <int CTRL, int RM = 0xf> DI float dppf(float v) {
    return __builtin_bit_cast(float, __builtin_amdgcn_update_dpp(0, __builtin_bit_cast(int, v), CTRL, RM, 0xf, true));
}
DI float rdl(float v, int l) { return __builtin_bit_cast(float, __builtin_amdgcn_readlane(__builtin_bit_cast(int, v), l)); }
DI void wave_sum2(float& a, float& b) {
    const auto sw = __builtin_amdgcn_permlane32_swap(__builtin_bit_cast(u32, a), __builtin_bit_cast(u32, b), false, false);
    float r = __builtin_bit_cast(float, (u32)sw[0]) + __builtin_bit_cast(float, (u32)sw[1]);
    r += dppf<0xB1>(r); r += dppf<0x4E>(r); r += dppf<0x141>(r); r += dppf<0x140>(r);
    asm volatile("s_nop 1\n\tv_add_f32_dpp %0, %0, %0 row_bcast:15 row_mask:0xa bank_mask:0xf\n\ts_nop 0" : "+v"(r));
    a = rdl(r, 31); b = rdl(r, 63);
}
template <int CTRL, int RM = 0xf> DI u32 dppu(u32 v) { return (u32)__builtin_amdgcn_update_dpp(0, (int)v, CTRL, RM, 0xf, true); }
DI u32 wave_maxu(u32 v) {
    v = max(v, dppu<0xB1>(v)); v = max(v, dppu<0x4E>(v)); v = max(v, dppu<0x141>(v)); v = max(v, dppu<0x140>(v));
    return max(max((u32)__builtin_amdgcn_readlane(v, 0), (u32)__builtin_amdgcn_readlane(v, 16)),
               max((u32)__builtin_amdgcn_readlane(v, 32), (u32)__builtin_amdgcn_readlane(v, 48)));
}
DI void ln_stats(const f2* x, float scale, float& rs, float& nb) {
    const float c = rdl(x[0].x, 0);
    f2 d[4];
#pragma unroll
    for (int i = 0; i < 4; ++i) d[i] = x[i] - sp2(c);
    const f2 s2 = (d[0] + d[1]) + (d[2] + d[3]);
    const f2 q2 = fma2(d[3], d[3], fma2(d[2], d[2], fma2(d[1], d[1], d[0] * d[0])));
    float s = s2.x + s2.y, q = q2.x + q2.y;
    wave_sum2(s, q);
    const float md = s * (1.0f / DM);
    const float var = fmaxf(fmaf(-md, md, q * (1.0f / DM)), 0.f);
    const float mb = rb(c + md);
    const float sd = rb(__builtin_amdgcn_sqrtf(rb(rb(var) + 1e-5f)));
    rs = __builtin_amdgcn_rcpf(sd) * scale;
    nb = -mb * rs;
}

DI bool has_sent(v4u v) { const u32 a = v.x > v.y ? v.x : v.y, b = v.z > v.w ? v.z : v.w; return (a > b ? a : b) == SENT; }
#define SB() __builtin_amdgcn_sched_barrier(0)
#define FENCE() do { asm volatile("" ::: "memory"); SB(); } while (0)
DI u32 now() { unsigned long long t_; asm volatile("s_memtime %0\n\ts_waitcnt lgkmcnt(0)" : "=s"(t_) :: "memory"); return (u32)t_; }
DI void wait_until(u32 t0, u32 d) {
#pragma unroll 1
    for (int i = 0; i < 512; ++i) if ((int)(now() - t0 - d) >= 0) break;
}
DI u32 rtime() { unsigned long long t_; asm volatile("s_memrealtime %0\n\ts_waitcnt lgkmcnt(0)" : "=s"(t_) :: "memory"); return (u32)t_; }
DI int wave_maxi(int v) {
    v = max(v, (int)dppu<0xB1>(v)); v = max(v, (int)dppu<0x4E>(v)); v = max(v, (int)dppu<0x141>(v)); v = max(v, (int)dppu<0x140>(v));
    return max(max(__builtin_amdgcn_readlane(v, 0), __builtin_amdgcn_readlane(v, 16)), max(__builtin_amdgcn_readlane(v, 32), __builtin_amdgcn_readlane(v, 48)));
}
DI v4u box_req(const unsigned char* base, u32 off) {
    v4u v; asm volatile("global_load_dwordx4 %0, %1, %2 sc1" : "=v"(v) : "v"(off), "s"(base) : "memory"); return v;
}
#define box_fin(N, v, base, off, id) box_fin_(N, v, base, off)
DI v4u box_fin_(int N, v4u v, const unsigned char* base, u32 off) {
    asm volatile("s_waitcnt vmcnt(%1)" : "+v"(v) : "i"(N) : "memory");
#pragma unroll 1
    for (u32 sp = 0; sp < (1u << 16); ++sp) {
        const unsigned long long bad = __builtin_amdgcn_ballot_w64(has_sent(v));
        if (!bad) break;
        unsigned long long sv;
        asm volatile("s_sleep 2\n\ts_mov_b64 %1, exec\n\ts_mov_b64 exec, %4\n\tglobal_load_dwordx4 %0, %2, %3 sc1\n\ts_mov_b64 exec, %1\n\ts_waitcnt vmcnt(0)"
                     : "+v"(v), "=&s"(sv) : "v"(off), "s"(base), "s"(bad) : "memory");
    }
    return v;
}
DI void box_put(v4u v, __amdgpu_buffer_rsrc_t r, u32 off, u32 soff) {
    asm volatile("" : "+v"(v));
    __builtin_amdgcn_raw_buffer_store_b128(v, r, off, soff, 16);
    asm volatile("s_nop 1" :: "v"(v) : "memory");
}
DI void glds16(const void* base, u32 off, u32 lds) {
    asm volatile("s_mov_b32 m0, %2\n\ts_nop 0\n\tglobal_load_lds_dwordx4 %0, %1" :: "v"(off), "s"(base), "s"(lds) : "memory");
}
DI void lds_bar() { asm volatile("s_waitcnt lgkmcnt(0)\n\ts_barrier" ::: "memory"); }
DI void vm_wait() { asm volatile("s_waitcnt vmcnt(0)" ::: "memory"); }

DI void write_copies(LAS unsigned char* kc  , const u32* d) {
    u32 o[5];
#pragma unroll
    for (int i = 0; i < 5; ++i) o[i] = __builtin_amdgcn_alignbit(d[i + 1], d[i], 16);
    *(LAS v4u*)(kc) = (v4u){d[2], d[3], d[4], d[5]};
    *(LAS v4u*)(kc + KC_S) = (v4u){o[1], o[2], o[3], o[4]};
    *(LAS v4u*)(kc + 2 * KC_S) = (v4u){d[1], d[2], d[3], d[4]};
    *(LAS v4u*)(kc + 3 * KC_S) = (v4u){o[0], o[1], o[2], o[3]};
}

template <bool BF> DI f4 mm(v4u a, v4u b, f4 c) {
    if constexpr (BF) return __builtin_amdgcn_mfma_f32_16x16x32_bf16(__builtin_bit_cast(bf8, a), __builtin_bit_cast(bf8, b), c, 0, 0, 0);
    else return __builtin_amdgcn_mfma_f32_16x16x32_f16(__builtin_bit_cast(h8, a), __builtin_bit_cast(h8, b), c, 0, 0, 0);
}
DI v2u tr_read(LAS const unsigned char* p) { return __builtin_bit_cast(v2u, __builtin_amdgcn_ds_read_tr16_b64_v4i16((LAS s4*)p)); }

template <int P0 = 0, int P1 = 8> DI void load_afrags(v4u (&a)[2][8], LAS const unsigned char* abase) {
#pragma unroll
    for (int p = P0; p < P1; ++p)
#pragma unroll
        for (int ri = 0; ri < 2; ++ri) {
            const v2u lo = tr_read(abase + 512 * p + 64 * ri + 8), hi = tr_read(abase + 512 * p + 64 * ri);
            a[ri][p] = (v4u){lo.x, lo.y, hi.x, hi.y};
        }
}
template <int Q0> DI void load_b(v4u (&bq)[2][4], LAS const unsigned char* bbase) {
#pragma unroll
    for (int q = 0; q < 4; ++q) {
#pragma unroll
        for (int ri = 0; ri < 2; ++ri) { bq[ri][q] = *(LAS const v4u*)(bbase + 512 * (Q0 + q) + 64 * (1 - ri)); SB(); }
    }
}
template <bool BF, int Q0, int q, int TA, int TB> DI void conv_q(const v4u (&a)[2][8], const v4u (&bq)[2][4], f4 (&acc)[8]) {
#pragma unroll
    for (int ri = 0; ri < 2; ++ri)
#pragma unroll
        for (int T = TA; T < TB; ++T) acc[T] = mm<BF>(a[ri][T - Q0 - q], bq[ri][q], acc[T]);
}
DI void put_tile(f4 v, LAS unsigned char* pbase, int T) { *(LAS f4*)(pbase + (T & 3) * 1024) = v; }
template <bool BF, int Q0, int PART> DI void conv_diag(const v4u (&a)[2][8], const v4u (&bq)[2][4], f4 (&acc)[8], LAS unsigned char* pbase) {
    if constexpr (PART == 0) {
        conv_q<BF, Q0, 0, Q0, Q0 + 4>(a, bq, acc);
        SB();
        conv_q<BF, Q0, 1, Q0 + 1, Q0 + 4>(a, bq, acc);
        SB();
        put_tile(acc[Q0], pbase, Q0);
        asm volatile("" : "+v"(acc[Q0 + 1]), "+v"(acc[Q0 + 2]), "+v"(acc[Q0 + 3]));
        SB();
    } else {
        conv_q<BF, Q0, 2, Q0 + 2, Q0 + 4>(a, bq, acc);
        SB();
        put_tile(acc[Q0 + 1], pbase, Q0 + 1);
        acc[Q0 + 3] = mm<BF>(a[0][0], bq[0][3], acc[Q0 + 3]);
        SB();
        put_tile(acc[Q0 + 2], pbase, Q0 + 2);
        acc[Q0 + 3] = mm<BF>(a[1][0], bq[1][3], acc[Q0 + 3]);
        SB();
    }
}
template <bool BF, int PART> DI void conv_off(const v4u (&a)[2][8], const v4u (&bq)[2][4], f4 (&acc)[8]) {
    conv_q<BF, 0, 2 * PART, 4, 8>(a, bq, acc);
    conv_q<BF, 0, 2 * PART + 1, 4, 8>(a, bq, acc);
    asm volatile("" : "+v"(acc[4]), "+v"(acc[5]), "+v"(acc[6]), "+v"(acc[7]));
    SB();
}
DI u32 pk_bf_s(float a, float b) { u32 r; asm("v_cvt_pk_bf16_f32 %0, %1, %2" : "=v"(r) : "v"(a), "v"(b)); return r; }
DI f4 sp4(float a) { return (f4){a, a, a, a}; }

DI f2 cmul(f2 a, f2 b) {
    f2 r;
    asm("v_pk_mul_f32 %0, %1, %2 op_sel:[0,0] op_sel_hi:[0,1]\n\t"
        "v_pk_fma_f32 %0, %1, %2, %0 op_sel:[1,1,0] op_sel_hi:[1,0,1] neg_lo:[0,1,0]" : "=&v"(r) : "v"(a), "v"(b));
    return r;
}
DI f2 cexp_p(float zr2, float fh, float fl, float p) {
    const float mag = __builtin_amdgcn_exp2f(zr2 * p);
    const float an = fmaf(fl, p, __builtin_amdgcn_fractf(fh * p));
    return (f2){__builtin_amdgcn_cosf(an), __builtin_amdgcn_sinf(an)} * sp2(mag);
}
DI float exp_acc(float x) {
    const float L = 1.44269504088896340736f, Ll = 1.92596299112661746e-08f;
    const float h = x * L;
    const float lo = fmaf(x, Ll, fmaf(x, L, -h));
    const float e = __builtin_amdgcn_exp2f(h);
    return fmaf(e * lo, 0.69314718055994530942f, e);
}

template <bool V> struct BC { static constexpr bool v = V; };
#define STAMP(i) do {} while (0)

__global__ void __launch_bounds__(512, 2)
k1_main(const u16* __restrict__ qin, const u16* __restrict__ kin, const u16* __restrict__ vin, const u16* __restrict__ shk,
        const u16* __restrict__ ssmk, const u16* __restrict__ ssmD, const u16* __restrict__ lar, const u16* __restrict__ aim,
        const u16* __restrict__ cre, const u16* __restrict__ cim, const u16* __restrict__ ldt, const u16* __restrict__ dsk,
        u16* __restrict__ out, unsigned char* ws) {
    extern __shared__ __attribute__((aligned(16))) unsigned char smem[];
    LAS unsigned char* L = (LAS unsigned char*)smem;
    const int t = threadIdx.x, l = t & 63;
    const int w = __builtin_amdgcn_readfirstlane(t >> 6);
    const int wg = (blockIdx.x & 7) * 32 + (blockIdx.x >> 3);
    const int H = (wg >> 1) & 1;
    auto body = [&](auto hc, auto wc) __attribute__((always_inline)) {
    constexpr bool HH = decltype(hc)::v, WC = decltype(wc)::v;
    asm volatile("" ::: "memory");
    const int rbk = HH * 128 + (wg >> 2) * 2 + (wg & 1);
    constexpr int c1 = WC;
    const int m = t & 255;
    const int rq = w & 3;
    const int n16 = l & 15, g = l >> 4;
    const __amdgpu_buffer_rsrc_t box = __builtin_amdgcn_make_buffer_rsrc(ws, 0, (int)(WS_FLAG + 32768), 0x00020000);
    STAMP(0);
    const v4u sent4 = {SENT, SENT, SENT, SENT};
    const u32 pub_row = (u32)((t >> 2) * 16384 + rbk * 64 + (t & 3) * 16);
    {
        const u32 vo = (u32)l * 16u;
        glds16(kin + (size_t)(8 * rbk + w) * DM, vo, (u32)(L_P + (3 + w) * KV_S));
        glds16(vin + (size_t)(8 * rbk + w) * DM, vo, (u32)(L_P + (11 + w) * KV_S));
        if (!WC && w < 3) {
            if (rbk > 0) glds16(kin + (size_t)(8 * rbk - 3 + w) * DM, vo, (u32)(L_P + w * KV_S));
            else *(LAS v4u*)(L + L_P + w * KV_S + l * 16) = (v4u){0u, 0u, 0u, 0u};
        }
    }
    const int cp = t >> 1, hh = t & 1;
    const v4u sw = *(const v4u*)(shk + cp * 8);
    FENCE();
    const int pch = 2 * wg + ((w >> 1) & 1), pn = t & 127;
    const u32 r_lar = lar[pch * NS + pn], r_aim = aim[pch * NS + pn], r_cre = cre[pch * NS + pn], r_cim = cim[pch * NS + pn];
    const u32 r_cre2 = cre[pch * NS + (pn ^ 64)], r_cim2 = cim[pch * NS + (pn ^ 64)];
    u32 vz; asm volatile("v_mov_b32 %0, 0" : "=v"(vz));
    const u32 r_ldt = ldt[pch + vz];
    const u32 dA2 = *(const u32*)(ssmD + 2 * wg + vz), dB2 = *(const u32*)(dsk + 2 * wg + vz);
    FENCE();
    const int chg = 2 * wg + c1;
    const v4u fcur = *(const v4u*)(ssmk + (size_t)chg * SEQ + 8 * m);
    v2u fprv = *(const v2u*)(ssmk + (size_t)chg * SEQ + (m > 0 ? 8 * m - 4 : 0));
    FENCE();
    const u32 ini_y = (u32)((rbk >> 1) * 16384 + w * 2048 + (rbk & 1) * 1024 + l * 16);
    const u32 ini_u = (u32)((wg >> 1) * 16384 + w * 2048 + (wg & 1) * 1024 + l * 16);
    box_put(sent4, box, ini_y, 1 * MB2);
    FENCE();
    const u32 c_s = now(), r_s = rtime();
    const int row = 8 * rbk + w;
    v2u nonce;
    {
        const unsigned long long id = amd_dispatch_id(), dp = (unsigned long long)__builtin_amdgcn_dispatch_ptr();
        const u32 a = (u32)id * 0x9E3779B1u + (u32)(dp >> 32);
        const u32 b = ((u32)dp ^ ((u32)(id >> 32) * 0x85EBCA77u)) & ~1u;
        nonce = (v2u){a, b | (~a & 1u)};
    }
    const u32 flag_w = t < 8 ? WS_FLAG + (u32)(t * 256 + rbk) * 16 : 0x7FFFFFF0u;
    FENCE();
    if (m < 30) *(LAS v4u*)(L + L_U + c1 * U_S + 16 * m) = (v4u){0u, 0u, 0u, 0u};
    if (m < 8) *(LAS v4u*)(L + L_KC + (c1 * 4 + (m >> 1)) * KC_S + (m & 1) * 16) = (v4u){0u, 0u, 0u, 0u};

    LAS const unsigned char* abase = L + L_KC + (c1 * 4 + (n16 >> 2)) * KC_S + 56 - 16 * g + 8 * (n16 & 3) + 128 * rq;
    LAS const unsigned char* bbase = L + L_U + c1 * U_S + 384 + 32 * n16 + 16 * g - 128 * rq;
    LAS unsigned char* pbase = L + L_P + (rq * 2 + c1) * 4096 + (g * 16 + ((n16 + 4 * g) & 15)) * 16;
    const int eg = l & 3, en = l >> 2;
    LAS const unsigned char* prd = L + L_P + rq * 1024 + (eg * 16 + ((en + 4 * eg) & 15)) * 16;
    LAS const unsigned char* urd = L + L_U + 480 + w * 512 + l * 8;
    LAS unsigned char* ust = L + L_U + (t & 1) * U_S + 480 + (t >> 1) * 16;
    LAS unsigned char* kcw = L + L_KC + c1 * 4 * KC_S + 32 + 16 * m;
    LAS u32* ti = (LAS u32*)(L + L_TI) + (4 * (t & 1)) * 260 + (t >> 1);
    LAS const unsigned char* tr = L + L_TO + ((l & 15) >> 2) * 1088 + (w * 64 + (l >> 4) * 16 + (l & 3) * 4) * 2;
    const u32 pub_col = (u32)((w * 16 + en) * 16384 + wg * 64 + eg * 16);
    const u32 get_p = (u32)((t >> 1) * 64 + (t & 1) * 16);
    const unsigned char* gu = ws + (wg >> 1) * 16384 + (wg & 1) * 32;
    const unsigned char* gy = ws + (rbk >> 1) * 16384 + (rbk & 1) * 32;
    const unsigned char* flag_b = ws + WS_FLAG + (wg >> 5) * 4096;
    const u32 flag_o = (u32)(t >> 1) * 16;

    {
        f2 w0 = {lo2f(sw.x), lo2f(sw.z)}, w1 = {hi2f(sw.x), hi2f(sw.z)}, w2 = {lo2f(sw.y), lo2f(sw.w)}, w3 = {hi2f(sw.y), hi2f(sw.w)};
        asm volatile("" : "+v"(w0), "+v"(w1), "+v"(w2), "+v"(w3));
        lds_bar();
        STAMP(1);
        LAS const unsigned char* kp = L + L_P + 4 * hh * KV_S + cp * 4;
        u32 kr[7], vr[4];
#pragma unroll
        for (int i = 0; i < 7; ++i) kr[i] = *(LAS const u32*)(kp + i * KV_S);
#pragma unroll
        for (int i = 0; i < 4; ++i) vr[i] = *(LAS const u32*)(kp + (11 + i) * KV_S);
        f2 kf[7], kv[4];
#pragma unroll
        for (int i = 0; i < 7; ++i) kf[i] = up2(kr[i]);
#pragma unroll
        for (int r = 0; r < 4; ++r) kv[r] = fma2(kf[r + 3], w0, fma2(kf[r + 2], w1, fma2(kf[r + 1], w2, kf[r] * w3))) * up2(vr[r]);
        const u32 p00 = pk_bf(kv[0].x, kv[1].x), p01 = pk_bf(kv[2].x, kv[3].x);
        const u32 p10 = pk_bf(kv[0].y, kv[1].y), p11 = pk_bf(kv[2].y, kv[3].y);
        const u32 s0 = hh ? p00 : p10, s1 = hh ? p01 : p11;
        const u32 r0 = dppu<0xB1>(s0), r1 = dppu<0xB1>(s1);
        v4u o = hh ? (v4u){r0, r1, p10, p11} : (v4u){p00, p01, r0, r1};
        asm volatile("" : "+v"(o));
            box_put(o, box, pub_row, 0);
        FENCE();
    }
    STAMP(2); STAMP(3);
    f2 C, e128, ea, eb;
    {
        const float dt = rb(exp_acc(bf2f(r_ldt)));
        const float are = rb(exp_acc(bf2f(r_lar)));
        const float zr2 = -(are * dt) * 1.44269504088896340736f;
        const float zi = bf2f(r_aim) * dt;
        const float chi = 0.15915494309189533577f, clo = 6.420638326565253e-09f;
        const float fh = zi * chi;
        const float fl2 = fmaf(zi, clo, fmaf(zi, chi, -fh));
        const u32 am = max(max(r_cre & 0x7FFFu, r_cim & 0x7FFFu), max(r_cre2 & 0x7FFFu, r_cim2 & 0x7FFFu));
        const float mx = bf2f(wave_maxu(am)) * dt;
        int ec = __builtin_amdgcn_frexp_expf(mx);
        ec = ec < -100 ? -100 : ec;
        const float sA = __builtin_amdgcn_ldexpf(1.0f, 13 - ec);
        C = (f2){bf2f(r_cre), -bf2f(r_cim)} * sp2(dt) * sp2(sA);
        e128 = cexp_p(zr2, -fh, -fl2, 128.f);
        ea = cexp_p(zr2, -fh, -fl2, WC ? 1.f : 4.f);
        eb = cexp_p(zr2, -fh, -fl2, WC ? 2.f : 512.f);
        if (l == 0) *(LAS int*)(L + L_KCMP + ((w >> 1) & 1) * KCMP_S + 2824) = ec;
        asm volatile("" : "+v"(C), "+v"(e128), "+v"(ea), "+v"(eb));
        SB();
    }
    STAMP(4);
    {
        if (m == 0) fprv = (v2u){0u, 0u};
        const u32 d[6] = {fprv.x, fprv.y, fcur.x, fcur.y, fcur.z, fcur.w};
        write_copies(kcw, d);
    }
    FENCE();
    v4u fl, q16;
    v4u af[2][8];
    v4u pc;
    {
        LAS unsigned char* tb = L + L_TB + ((w >> 1) & 1) * TB_S + pn * 4;
#define PUT(rowi, v) *(LAS u32*)(tb + (rowi) * TB_R) = pk_h((v).x, (v).y)
#define MIDDLE() do { \
        SB(); \
        vm_wait();                 \
        lds_bar(); \
        if (!WC && w == 0) box_put((v4u){nonce.x, nonce.y, rtime(), 0u}, box, flag_w, 0); \
        tfl = now(); \
        q16 = *(const v4u*)(qin + (size_t)row * DM + 8 * l); \
        FENCE(); \
        STAMP(5); \
        load_afrags(af, abase); } while (0)
#define MIDDLE2() do { \
        SB(); \
        wait_until(tfl, D_FL + (31 - (blockIdx.x >> 3)) * FL_K); \
        fl = box_req(flag_b, flag_o); \
        FENCE(); } while (0)
        u32 tfl;
        if constexpr (!WC) {
            f2 G = cmul(C, eb);
            f2 e8 = cmul(ea, ea); e8.y = -e8.y;
            f2 B = {16384.f, 0.f};
#pragma unroll
            for (int r = 0; r < 16; ++r) {
                const int h = r + 4;
                if (r == MID_A) { asm volatile("" : "+v"(G), "+v"(B), "+v"(e8)); MIDDLE(); }
                if (r == MID_B) { asm volatile("" : "+v"(G), "+v"(B), "+v"(e8)); MIDDLE2(); }
                PUT(41 + r, B); if (r < 15) B = cmul(B, e8);
                if (h < 8) { PUT(24 + 2 * (h - 4), G); const f2 G4 = cmul(G, ea); PUT(25 + 2 * (h - 4), G4); }
                else if (h <= 16) PUT(24 + h, G);
                if (h < 16) G = cmul(G, e128);
            }
        } else {
            f2 e[8];
            e[1] = ea; e[2] = eb; e[3] = cmul(e[2], e[1]); e[4] = cmul(e[2], e[2]);
            e[5] = cmul(e[4], e[1]); e[6] = cmul(e[4], e[2]); e[7] = cmul(e[4], e[3]);
            f2 G = C;
#pragma unroll
            for (int h = 0; h < 4; ++h) {
                if (h == MID_C) { asm volatile("" : "+v"(G)); MIDDLE(); }
                if (h == MID_D) { asm volatile("" : "+v"(G)); MIDDLE2(); }
                if (h < 2) {
                    PUT(8 * h, G);
#pragma unroll
                    for (int a = 1; a < 8; ++a) { const f2 v = cmul(G, e[a]); PUT(8 * h + a, v); }
                } else {
                    PUT(16 + 4 * (h - 2), G);
#pragma unroll
                    for (int a = 1; a < 4; ++a) { const f2 v = cmul(G, e[2 * a]); PUT(16 + 4 * (h - 2) + a, v); }
                }
                if (h < 3) G = cmul(G, e128);
            }
        }
#undef PUT
    }
    STAMP(6);

    f4 acc[8];
    v4u bq[2][4];
    v4u py, py2;
#define EPILOGUE(BOXI, S0, S1) do { \
        f4 pp[8]; \
        _Pragma("unroll") for (int i = 0; i < 8; ++i) pp[i] = *(LAS const f4*)(prd + i * 4096); \
        const f4 y0 = __builtin_elementwise_fma((pp[0] + pp[2]) + (pp[4] + pp[6]), sp4(S0), e0); \
        const f4 y1 = __builtin_elementwise_fma((pp[1] + pp[3]) + (pp[5] + pp[7]), sp4(S1), e1); \
        v4u o; \
        _Pragma("unroll") for (int r = 0; r < 4; ++r) o[r] = clampS(pk_bf_s(y0[r], y1[r])); \
        box_put(o, box, pub_col, (BOXI) * MB2); \
        FENCE(); \
        __builtin_amdgcn_s_setprio(0); } while (0)
#define ZERO_ACC() do { _Pragma("unroll") for (int i = 0; i < 8; ++i) acc[i] = (f4){0.f, 0.f, 0.f, 0.f}; } while (0)
#define PUT_TI(p, o) do { ti[(o)] = p.x; ti[260 + (o)] = p.y; ti[520 + (o)] = p.z; ti[780 + (o)] = p.w; } while (0)
#define KGEN_MM(ch, mt, bfr) do { \
        LAS const unsigned char* ta = L + L_TB + (ch) * TB_S + (16 * (mt) + n16) * TB_R + 16 * g; \
        v4u afr[8]; \
        _Pragma("unroll") for (int s = 0; s < 8; ++s) afr[s] = *(LAS const v4u*)(ta + 64 * s); \
        f4 d0 = {0.f, 0.f, 0.f, 0.f}, d1 = {0.f, 0.f, 0.f, 0.f}; \
        _Pragma("unroll") for (int s = 0; s < 8; s += 2) { d0 = mm<false>(afr[s], bfr[s], d0); d1 = mm<false>(afr[s + 1], bfr[s + 1], d1); } \
        const f4 dacc = d0 + d1; \
        const int pb = 8 * n16 + ((mt) == 0 ? 4 * (g & 1) + 128 * (g >> 1) : (mt) == 1 ? (g < 2 ? 256 + 128 * g : 256 * g) : 1024 + 512 * g); \
        const int s1 = (mt) == 0 ? 1 : (mt) == 1 ? (g < 2 ? 2 : 4) : 128, s2 = (mt) == 0 ? 2 : (mt) == 1 ? (g < 2 ? 4 : 128) : 256; \
        _Pragma("unroll") for (int i = 0; i < 4; ++i) { \
            int p = pb + (i & 1) * s1 + (i >> 1) * s2; \
            p = p > 2048 ? 2064 : p; \
            u32 ad = __builtin_bit_cast(u32, (float)p) >> 15; \
            if (i == 0) ad = ad > 0x7EFEu ? ad : 0x7EFEu; \
            *(LAS _Float16*)(L + L_KCMP + (ch) * KCMP_S - 0x7EFE + ad) = (_Float16)(dacc[i] * 1.9073486328125e-06f);     \
        } } while (0)
#define EXPAND() do { \
        u32 d[6]; \
        const float f0 = (float)(8 * m - 4); \
        const u32 kb = (u32)(L_KCMP + c1 * KCMP_S - 2 * 0x3F7F); \
        _Pragma("unroll") for (int i = 0; i < 6; ++i) { \
            float fa = f0 + (float)(2 * i), fb = f0 + (float)(2 * i + 1); \
            if (i < 3) fa = fmaxf(fa, 0.99609375f); \
            if (i < 2) fb = fmaxf(fb, 0.99609375f); \
            const u32 cd = pk_bf(fa, fb); \
            u32 a0, a1; \
            asm("v_mad_u32_u16 %0, %2, 2, %3\n\tv_mad_u32_u16 %1, %2, 2, %3 op_sel:[1,0,0,0]" : "=&v"(a0), "=v"(a1) : "v"(cd), "s"(kb)); \
            us2 v; v.x = *(LAS const u16*)(L + a0); v.y = *(LAS const u16*)(L + a1); \
            d[i] = __builtin_bit_cast(u32, v); \
        } \
        if (m == 0) { d[0] = 0u; d[1] = 0u; } \
        write_copies(kcw, d); \
        SB(); } while (0)
    {
        asm volatile("s_waitcnt vmcnt(0)" : "+v"(fl) :: "memory");
#pragma unroll 1
        for (u32 sp = 0; sp < (1u << 16); ++sp) {
            const unsigned long long bad = __builtin_amdgcn_ballot_w64(fl.x != nonce.x || fl.y != nonce.y);
            if (!bad) break;
            unsigned long long sv;
            asm volatile("s_sleep 1\n\ts_mov_b64 %1, exec\n\ts_mov_b64 exec, %4\n\tglobal_load_dwordx4 %0, %2, %3 sc1\n\ts_mov_b64 exec, %1\n\ts_waitcnt vmcnt(0)"
                         : "+v"(fl), "=&s"(sv) : "v"(flag_o), "s"(flag_b), "s"(bad) : "memory");
        }
        asm volatile("" : "+v"(q16));
        pc = box_req(gu, get_p);
        FENCE();
        STAMP(7);
    }
    lds_bar();
    if (w < 6) {
        const int ch = w >= 3;
        LAS const unsigned char* tbb = L + L_TB + ch * TB_S + (41 + n16) * TB_R + 16 * g;
        v4u bfr[8];
#pragma unroll
        for (int s = 0; s < 8; ++s) bfr[s] = *(LAS const v4u*)(tbb + 64 * s);
        const int mt = w - 3 * ch;
        KGEN_MM(ch, mt, bfr);
    }
    SB();
    {
        const u32 c_n = now(), r_n = rtime();
        const float cpt = (float)(c_n - c_s) * __builtin_amdgcn_rcpf((float)(r_n - r_s));
        const u32 epw = c_n + (u32)(int)((float)wave_maxi((int)(fl.z - r_n)) * cpt);
        if (l == 0) *(LAS u32*)(L + L_EP + 4 * w) = epw;
    }
    SB();
    float rs0, nb0;
    u32 ep;
    {
        STAMP(8);
        asm volatile("s_waitcnt vmcnt(0)" : "+v"(pc) :: "memory");
        *(LAS v4u*)ust = pc;
        box_put(sent4, box, ini_u, 2 * MB2);
        box_put(sent4, box, ini_y, 3 * MB2);
        lds_bar();
        STAMP(9);
        load_b<0>(bq, bbase);
        const v2u i0 = *(LAS const v2u*)(urd), i1 = *(LAS const v2u*)(urd + U_S);
        ZERO_ACC();
        SB();
        conv_diag<true, 0, 0>(af, bq, acc, pbase);
        conv_diag<true, 0, 1>(af, bq, acc, pbase);
        f4 e0 = (f4){lo2f(i0.x), hi2f(i0.x), lo2f(i0.y), hi2f(i0.y)} * sp4(lo2f(dA2));
        f4 e1 = (f4){lo2f(i1.x), hi2f(i1.x), lo2f(i1.y), hi2f(i1.y)} * sp4(hi2f(dA2));
        asm volatile("" : "+v"(e0), "+v"(e1));
        SB();
        put_tile(acc[3], pbase, 3);
        if constexpr (!WC) {
            __builtin_amdgcn_s_setprio(3);
            asm volatile("s_waitcnt vmcnt(1)" ::: "memory");
            lds_bar();
            EPILOGUE(1, 1.0f, 1.0f);
        } else {
            lds_bar();
        }
        STAMP(10);
        {
            const v4u ea_ = *(LAS const v4u*)(L + L_EP), eb_ = *(LAS const v4u*)(L + L_EP + 16);
            const u32 r = ea_.x;
            const int d = max(max(max((int)(ea_.y - r), (int)(ea_.z - r)), max((int)(ea_.w - r), (int)(eb_.x - r))),
                              max(max((int)(eb_.y - r), (int)(eb_.z - r)), max((int)(eb_.w - r), 0)));
            ep = __builtin_amdgcn_readfirstlane(r + (u32)d);
        }
        v4u b2[2][4];
        conv_off<true, 0>(af, bq, acc);
        load_b<4>(b2, bbase);
        conv_off<true, 1>(af, bq, acc);
        lds_bar();
        STAMP(11);
        conv_diag<true, 4, 0>(af, b2, acc, pbase);
        if constexpr (!HH && !WC) { wait_until(ep, S_YA0); py = box_req(gy + 1 * MB2, get_p); py2 = box_req(gy + 1 * MB2 + 8192, get_p); SB(); }
        conv_diag<true, 4, 1>(af, b2, acc, pbase);
        put_tile(acc[7], pbase, 7);
        if constexpr (WC) { __builtin_amdgcn_s_setprio(3); asm volatile("s_waitcnt vmcnt(1)" ::: "memory"); }
        lds_bar();
        if constexpr (WC) EPILOGUE(1, 1.0f, 1.0f);
        STAMP(12);
        box_put(sent4, box, ini_u, 4 * MB2);
        FENCE();
        const f2 xq[4] = {up2(q16.x), up2(q16.y), up2(q16.z), up2(q16.w)};
        ln_stats(xq, 1.0f, rs0, nb0);
    }
    const float sc0 = __builtin_amdgcn_ldexpf(1.0f / USCALE, *(LAS const int*)(L + L_KCMP + 2824) - 7);
    const float sc1 = __builtin_amdgcn_ldexpf(1.0f / USCALE, *(LAS const int*)(L + L_KCMP + KCMP_S + 2824) - 7);

    v4u yAp;
    f2 x[4] = {up2(q16.x), up2(q16.y), up2(q16.z), up2(q16.w)};
#pragma unroll
    for (int it = 0; it < 3; ++it) {
        {
            f2 gate[4];
            if (it == 0) {
#pragma unroll
                for (int i = 0; i < 4; ++i) gate[i] = fma2(x[i], sp2(rs0), sp2(nb0));
            } else if (it == 1) { gate[0] = up2(yAp.x); gate[1] = up2(yAp.y); gate[2] = up2(yAp.z); gate[3] = up2(yAp.w); }
            if (it < 2) asm volatile("" : "+v"(gate[0]), "+v"(gate[1]), "+v"(gate[2]), "+v"(gate[3]));
            if constexpr (HH) {
                if (it == 0) { EXPAND(); lds_bar(); }
                wait_until(ep, it == 0 ? S_YA1 : it == 1 ? S_Y10 : S_Y11);
                py = box_req(gy + (1 + 2 * it) * MB2, get_p);
                if (it == 0) { SB(); load_afrags(af, abase); SB(); }
                py = box_fin(0, py, gy + (1 + 2 * it) * MB2, get_p, 1 + 4 * it);
                PUT_TI(py, 0);
                if (it < 2 && !WC) { wait_until(ep, it ? S_U01 : S_U00); pc = box_req(gu + (2 + 2 * it) * MB2, get_p); }
            } else if constexpr (!WC) {
                py = box_fin(it == 0 ? 2 : 1, py, gy + (1 + 2 * it) * MB2, get_p, 4 * it);
                PUT_TI(py, 0);
                py2 = box_fin(it == 0 ? 1 : 0, py2, gy + (1 + 2 * it) * MB2 + 8192, get_p, 4 * it);
                PUT_TI(py2, 128);
            } else {
                asm volatile("s_waitcnt vmcnt(%0)" :: "i"(it == 0 ? 2 : 1) : "memory");
            }
            lds_bar();
            STAMP(13 + 7 * it);
            const v4u yv = *(LAS const v4u*)(L + L_TI + w * 1040 + l * 16);
            const f2 yc[4] = {up2(yv.x), up2(yv.y), up2(yv.z), up2(yv.w)};
            float rs, nb;
            if (it == 0) {
#pragma unroll
                for (int i = 0; i < 4; ++i) x[i] = fma2(gate[i], yc[i], x[i]);
                yAp = yv;
            } else {
#pragma unroll
                for (int i = 0; i < 4; ++i) x[i] += yc[i];
            }
            if (it == 2) {
                *(v4u*)(out + (size_t)row * DM + 8 * l) = (v4u){pk_bf(x[0].x, x[0].y), pk_bf(x[1].x, x[1].y), pk_bf(x[2].x, x[2].y), pk_bf(x[3].x, x[3].y)};
                STAMP(14 + 7 * it);
                break;
            }
            if (it == 1) {
                ln_stats(x, 1.0f, rs, nb);
#pragma unroll
                for (int i = 0; i < 4; ++i) x[i] = fma2(fma2(x[i], sp2(rs), sp2(nb)), gate[i], x[i]);
            }
            ln_stats(x, USCALE, rs, nb);
            v4u uo;
#pragma unroll
            for (int i = 0; i < 4; ++i) { const f2 u = fma2(x[i], sp2(rs), sp2(nb)); uo[i] = pk_h(u.x, u.y); }
            *(LAS v4u*)(L + L_TO + w * 1088 + l * 16) = uo;
            lds_bar();
            const v2u lo2 = tr_read(tr), hi2 = tr_read(tr + 4 * 1088);
            box_put((v4u){clampS(lo2.x), clampS(lo2.y), clampS(hi2.x), clampS(hi2.y)}, box, pub_row, (2 + 2 * it) * MB2);
            FENCE();
            STAMP(14 + 7 * it);
        }
        {
            if constexpr (HH) {
                if constexpr (!WC) { pc = box_fin(1, pc, gu + (2 + 2 * it) * MB2, get_p, 2 + 4 * it); *(LAS v4u*)ust = pc; }
            } else {
                if (it == 0) { EXPAND(); lds_bar(); }
                if constexpr (!WC) { wait_until(ep, it ? S_U01 : S_U00); pc = box_req(gu + (2 + 2 * it) * MB2, get_p); }
                if (it == 0) { SB(); load_afrags(af, abase); SB(); }
                if constexpr (!WC) { pc = box_fin(0, pc, gu + (2 + 2 * it) * MB2, get_p, 2 + 4 * it); *(LAS v4u*)ust = pc; }
            }
            lds_bar();
            STAMP(15 + 7 * it);
            load_b<0>(bq, bbase);
            const float d0 = lo2f(dB2) * (1.0f / USCALE), d1 = hi2f(dB2) * (1.0f / USCALE);
#define SKIP_LOAD() const v2u i0 = *(LAS const v2u*)(urd), i1 = *(LAS const v2u*)(urd + U_S)
#define SKIP_TERMS() \
            const h2 h00 = as_h2(i0.x), h01 = as_h2(i0.y), h10 = as_h2(i1.x), h11 = as_h2(i1.y); \
            f4 e0 = (f4){(float)h00.x, (float)h00.y, (float)h01.x, (float)h01.y} * sp4(d0); \
            f4 e1 = (f4){(float)h10.x, (float)h10.y, (float)h11.x, (float)h11.y} * sp4(d1); \
            asm volatile("" : "+v"(e0), "+v"(e1)); SB()
            ZERO_ACC();
            if constexpr (!WC) {
                SKIP_LOAD();
                SB();
                conv_diag<false, 0, 0>(af, bq, acc, pbase);
                conv_diag<false, 0, 1>(af, bq, acc, pbase);
                SKIP_TERMS();
                put_tile(acc[3], pbase, 3);
                __builtin_amdgcn_s_setprio(3);
                lds_bar();
                STAMP(16 + 7 * it);
                EPILOGUE(3 + 2 * it, sc0, sc1);
            } else {
                SB();
                conv_diag<false, 0, 0>(af, bq, acc, pbase);
                conv_diag<false, 0, 1>(af, bq, acc, pbase);
                put_tile(acc[3], pbase, 3);
                if constexpr (HH) { wait_until(ep, it ? S_U11 : S_U10); pc = box_req(gu + (2 + 2 * it) * MB2, get_p); }
                lds_bar();
                STAMP(16 + 7 * it);
            }
            if (it == 0 && !(HH && WC)) {
                box_put(sent4, box, ini_y, 5 * MB2);
                if (!WC && w == 0) __builtin_amdgcn_raw_buffer_store_b64(~nonce, box, flag_w, 0, 16);
                FENCE();
            }
            conv_off<false, 0>(af, bq, acc);
            if constexpr (WC && !HH) { wait_until(ep, it ? S_U11 : S_U10); pc = box_req(gu + (2 + 2 * it) * MB2, get_p); SB(); }
            conv_off<false, 1>(af, bq, acc);
            if constexpr (WC) {
                pc = box_fin(0, pc, gu + (2 + 2 * it) * MB2, get_p, 3 + 4 * it);
                *(LAS v4u*)ust = pc;
                if (it == 0 && HH) {
                    box_put(sent4, box, ini_y, 5 * MB2);
                    FENCE();
                }
            }
            STAMP(17 + 7 * it);
            lds_bar();
            STAMP(18 + 7 * it);
            load_b<4>(bq, bbase);
            if constexpr (WC) {
                SKIP_LOAD();
                SB();
                conv_diag<false, 4, 0>(af, bq, acc, pbase);
                conv_diag<false, 4, 1>(af, bq, acc, pbase);
                SKIP_TERMS();
                put_tile(acc[7], pbase, 7);
                __builtin_amdgcn_s_setprio(3);
                lds_bar();
                EPILOGUE(3 + 2 * it, sc0, sc1);
            } else {
                conv_diag<false, 4, 0>(af, bq, acc, pbase);
                if constexpr (!HH) {
                    wait_until(ep, it ? S_Y01 : S_Y00);
                    py = box_req(gy + (3 + 2 * it) * MB2, get_p); py2 = box_req(gy + (3 + 2 * it) * MB2 + 8192, get_p); SB();
                }
                conv_diag<false, 4, 1>(af, bq, acc, pbase);
                put_tile(acc[7], pbase, 7);
                lds_bar();
            }
            STAMP(19 + 7 * it);
        }
    }
    };
    if (H) { if (w < 4) body(BC<true>{}, BC<false>{}); else body(BC<true>{}, BC<true>{}); }
    else   { if (w < 4) body(BC<false>{}, BC<false>{}); else body(BC<false>{}, BC<true>{}); }
}

extern "C" void kernel_launch(void* const* d_in, const int* in_sizes, int n_in, void* d_out, int out_size, void* d_ws, size_t ws_size,
                              hipStream_t stream) {
    (void)hipFuncSetAttribute((const void*)k1_main, hipFuncAttributeMaxDynamicSharedMemorySize, L_END);
    k1_main<<<256, 512, L_END, stream>>>((const u16*)d_in[0], (const u16*)d_in[1], (const u16*)d_in[2], (const u16*)d_in[3], (const u16*)d_in[4],
                                         (const u16*)d_in[5], (const u16*)d_in[7], (const u16*)d_in[8], (const u16*)d_in[9], (const u16*)d_in[10],
                                         (const u16*)d_in[11], (const u16*)d_in[12], (u16*)d_out, (unsigned char*)d_ws);
}
```

```cpp
#include <hip/hip_runtime.h>

typedef unsigned short u16;
typedef unsigned u32;
typedef u32 v4u __attribute__((ext_vector_type(4)));
typedef u32 v2u __attribute__((ext_vector_type(2)));
typedef float f4 __attribute__((ext_vector_type(4)));
typedef float f2 __attribute__((ext_vector_type(2)));
typedef __bf16 bf8 __attribute__((ext_vector_type(8)));
typedef __bf16 bf2 __attribute__((ext_vector_type(2)));
typedef _Float16 h8 __attribute__((ext_vector_type(8)));
typedef _Float16 h2 __attribute__((ext_vector_type(2)));
typedef short s4 __attribute__((ext_vector_type(4)));
typedef unsigned short us2 __attribute__((ext_vector_type(2)));
#define LAS __attribute__((address_space(3)))
#define DI __device__ __forceinline__
extern "C" __device__ unsigned long long amd_dispatch_id() __asm("llvm.amdgcn.dispatch.id");

constexpr int SEQ = 2048, DM = 512, NS = 128;
constexpr u32 SENT = 0xFFFFFFFFu;
constexpr u32 MB2 = 2u * 1024u * 1024u;
constexpr u32 WS_FLAG = 6 * MB2;
constexpr size_t WS_STAMP = 8 * (size_t)MB2;
constexpr float USCALE = 16.0f;
#define D_FL 700
#define MID_A 0
#define MID_B 8
#define MID_C 0
#define MID_D 2
#define FL_K 25
#define DLY 0
#define PF_LEN 58000
#define INI_EARLY 1
#define S_YA0 6500
#define S_YA1 9000
#define S_U00 11000
#define S_U10 13500
#define S_Y00 15500
#define S_Y10 18000
#define S_U01 20500
#define S_U11 23000
#define S_Y01 25000
#define S_Y11 27500

constexpr int KC_S = 4160;
constexpr int L_KC = 0;
constexpr int U_S = 4672;
constexpr int L_U = L_KC + 2 * 4 * KC_S;
constexpr int L_TI = L_U + 2 * U_S;
constexpr int L_TO = L_TI + 8 * 1040;
constexpr int KCMP_S = 2832;
constexpr int L_KCMP = L_TO + 8 * 1088;
constexpr int TB_R = 544, TB_S = 57 * TB_R;
constexpr int L_TB = L_KCMP + 2 * KCMP_S;
constexpr int L_P = L_TB + 2 * TB_S;
constexpr int KV_S = 1056;
constexpr int L_EP = L_P + 32768;
constexpr int L_END = L_EP + 64;

DI float bf2f(u32 h) { return __builtin_bit_cast(float, h << 16); }
DI float lo2f(u32 d) { return __builtin_bit_cast(float, d << 16); }
DI float hi2f(u32 d) { return __builtin_bit_cast(float, d & 0xFFFF0000u); }
DI f2 up2(u32 d) { return (f2){lo2f(d), hi2f(d)}; }
DI float rb(float x) { f2 t = {0.f, x}; return __builtin_bit_cast(float, __builtin_convertvector(t, bf2)); }
DI u32 pk_bf(float a, float b) { f2 t = {a, b}; return __builtin_bit_cast(u32, __builtin_convertvector(t, bf2)); }
DI u32 pk_h(float a, float b) { f2 t = {a, b}; return __builtin_bit_cast(u32, __builtin_convertvector(t, h2)); }
DI h2 as_h2(u32 d) { return __builtin_bit_cast(h2, d); }
DI u32 clampS(u32 d) { return d < 0xFFFFFFFEu ? d : 0xFFFFFFFEu; }
DI f2 fma2(f2 a, f2 b, f2 c) { return __builtin_elementwise_fma(a, b, c); }
DI f2 sp2(float a) { return (f2){a, a}; }
template <int CTRL, int RM = 0xf> DI float dppf(float v) {
    return __builtin_bit_cast(float, __builtin_amdgcn_update_dpp(0, __builtin_bit_cast(int, v), CTRL, RM, 0xf, true));
}
DI float rdl(float v, int l) { return __builtin_bit_cast(float, __builtin_amdgcn_readlane(__builtin_bit_cast(int, v), l)); }
DI void wave_sum2(float& a, float& b) {
    const auto sw = __builtin_amdgcn_permlane32_swap(__builtin_bit_cast(u32, a), __builtin_bit_cast(u32, b), false, false);
    float r = __builtin_bit_cast(float, (u32)sw[0]) + __builtin_bit_cast(float, (u32)sw[1]);
    r += dppf<0xB1>(r); r += dppf<0x4E>(r); r += dppf<0x141>(r); r += dppf<0x140>(r);
    asm volatile("s_nop 1\n\tv_add_f32_dpp %0, %0, %0 row_bcast:15 row_mask:0xa bank_mask:0xf\n\ts_nop 0" : "+v"(r));
    a = rdl(r, 31); b = rdl(r, 63);
}
template <int CTRL, int RM = 0xf> DI u32 dppu(u32 v) { return (u32)__builtin_amdgcn_update_dpp(0, (int)v, CTRL, RM, 0xf, true); }
DI u32 wave_maxu(u32 v) {
    v = max(v, dppu<0xB1>(v)); v = max(v, dppu<0x4E>(v)); v = max(v, dppu<0x141>(v)); v = max(v, dppu<0x140>(v));
    return max(max((u32)__builtin_amdgcn_readlane(v, 0), (u32)__builtin_amdgcn_readlane(v, 16)),
               max((u32)__builtin_amdgcn_readlane(v, 32), (u32)__builtin_amdgcn_readlane(v, 48)));
}
DI void ln_stats(const f2* x, float scale, float& rs, float& nb) {
    const float c = rdl(x[0].x, 0);
    f2 d[4];
#pragma unroll
    for (int i = 0; i < 4; ++i) d[i] = x[i] - sp2(c);
    const f2 s2 = (d[0] + d[1]) + (d[2] + d[3]);
    const f2 q2 = fma2(d[3], d[3], fma2(d[2], d[2], fma2(d[1], d[1], d[0] * d[0])));
    float s = s2.x + s2.y, q = q2.x + q2.y;
    wave_sum2(s, q);
    const float md = s * (1.0f / DM);
    const float var = fmaxf(fmaf(-md, md, q * (1.0f / DM)), 0.f);
    const float mb = rb(c + md);
    const float sd = rb(__builtin_amdgcn_sqrtf(rb(rb(var) + 1e-5f)));
    rs = __builtin_amdgcn_rcpf(sd) * scale;
    nb = -mb * rs;
}

DI bool has_sent(v4u v) { const u32 a = v.x > v.y ? v.x : v.y, b = v.z > v.w ? v.z : v.w; return (a > b ? a : b) == SENT; }
#define SB() __builtin_amdgcn_sched_barrier(0)
#define FENCE() do { asm volatile("" ::: "memory"); SB(); } while (0)
DI u32 now() { unsigned long long t_; asm volatile("s_memtime %0\n\ts_waitcnt lgkmcnt(0)" : "=s"(t_) :: "memory"); return (u32)t_; }
DI void wait_until(u32 t0, u32 d) {
#pragma unroll 1
    for (int i = 0; i < 512; ++i) if ((int)(now() - t0 - d) >= 0) break;
}
DI u32 rtime() { unsigned long long t_; asm volatile("s_memrealtime %0\n\ts_waitcnt lgkmcnt(0)" : "=s"(t_) :: "memory"); return (u32)t_; }
DI int wave_maxi(int v) {
    v = max(v, (int)dppu<0xB1>(v)); v = max(v, (int)dppu<0x4E>(v)); v = max(v, (int)dppu<0x141>(v)); v = max(v, (int)dppu<0x140>(v));
    return max(max(__builtin_amdgcn_readlane(v, 0), __builtin_amdgcn_readlane(v, 16)), max(__builtin_amdgcn_readlane(v, 32), __builtin_amdgcn_readlane(v, 48)));
}
DI v4u box_req(const unsigned char* base, u32 off) {
    v4u v; asm volatile("global_load_dwordx4 %0, %1, %2 sc1" : "=v"(v) : "v"(off), "s"(base) : "memory"); return v;
}
#define box_fin(N, v, base, off, id) box_fin_(N, v, base, off)
DI v4u box_fin_(int N, v4u v, const unsigned char* base, u32 off) {
    asm volatile("s_waitcnt vmcnt(%1)" : "+v"(v) : "i"(N) : "memory");
#pragma unroll 1
    for (u32 sp = 0; sp < (1u << 16); ++sp) {
        const unsigned long long bad = __builtin_amdgcn_ballot_w64(has_sent(v));
        if (!bad) break;
        unsigned long long sv;
        asm volatile("s_sleep 2\n\ts_mov_b64 %1, exec\n\ts_mov_b64 exec, %4\n\tglobal_load_dwordx4 %0, %2, %3 sc1\n\ts_mov_b64 exec, %1\n\ts_waitcnt vmcnt(0)"
                     : "+v"(v), "=&s"(sv) : "v"(off), "s"(base), "s"(bad) : "memory");
    }
    return v;
}
DI void box_put(v4u v, __amdgpu_buffer_rsrc_t r, u32 off, u32 soff) {
    asm volatile("" : "+v"(v));
    __builtin_amdgcn_raw_buffer_store_b128(v, r, off, soff, 16);
    asm volatile("s_nop 1" :: "v"(v) : "memory");
}
DI void glds16(const void* base, u32 off, u32 lds) {
    asm volatile("s_mov_b32 m0, %2\n\ts_nop 0\n\tglobal_load_lds_dwordx4 %0, %1" :: "v"(off), "s"(base), "s"(lds) : "memory");
}
DI void lds_bar() { asm volatile("s_waitcnt lgkmcnt(0)\n\ts_barrier" ::: "memory"); }
DI void vm_wait() { asm volatile("s_waitcnt vmcnt(0)" ::: "memory"); }

DI void write_copies(LAS unsigned char* kc  , const u32* d) {
    u32 o[5];
#pragma unroll
    for (int i = 0; i < 5; ++i) o[i] = __builtin_amdgcn_alignbit(d[i + 1], d[i], 16);
    *(LAS v4u*)(kc) = (v4u){d[2], d[3], d[4], d[5]};
    *(LAS v4u*)(kc + KC_S) = (v4u){o[1], o[2], o[3], o[4]};
    *(LAS v4u*)(kc + 2 * KC_S) = (v4u){d[1], d[2], d[3], d[4]};
    *(LAS v4u*)(kc + 3 * KC_S) = (v4u){o[0], o[1], o[2], o[3]};
}

template <bool BF> DI f4 mm(v4u a, v4u b, f4 c) {
    if constexpr (BF) return __builtin_amdgcn_mfma_f32_16x16x32_bf16(__builtin_bit_cast(bf8, a), __builtin_bit_cast(bf8, b), c, 0, 0, 0);
    else return __builtin_amdgcn_mfma_f32_16x16x32_f16(__builtin_bit_cast(h8, a), __builtin_bit_cast(h8, b), c, 0, 0, 0);
}
DI v2u tr_read(LAS const unsigned char* p) { return __builtin_bit_cast(v2u, __builtin_amdgcn_ds_read_tr16_b64_v4i16((LAS s4*)p)); }

template <int P0 = 0, int P1 = 8> DI void load_afrags(v4u (&a)[2][8], LAS const unsigned char* abase) {
#pragma unroll
    for (int p = P0; p < P1; ++p)
#pragma unroll
        for (int ri = 0; ri < 2; ++ri) {
            const v2u lo = tr_read(abase + 512 * p + 64 * ri + 8), hi = tr_read(abase + 512 * p + 64 * ri);
            a[ri][p] = (v4u){lo.x, lo.y, hi.x, hi.y};
        }
}
template <int Q0> DI void load_b(v4u (&bq)[2][4], LAS const unsigned char* bbase) {
#pragma unroll
    for (int q = 0; q < 4; ++q) {
#pragma unroll
        for (int ri = 0; ri < 2; ++ri) { bq[ri][q] = *(LAS const v4u*)(bbase + 512 * (Q0 + q) + 64 * (1 - ri)); SB(); }
    }
}
template <bool BF, int Q0, int q, int TA, int TB> DI void conv_q(const v4u (&a)[2][8], const v4u (&bq)[2][4], f4 (&acc)[8]) {
#pragma unroll
    for (int ri = 0; ri < 2; ++ri)
#pragma unroll
        for (int T = TA; T < TB; ++T) acc[T] = mm<BF>(a[ri][T - Q0 - q], bq[ri][q], acc[T]);
}
DI void put_tile(f4 v, LAS unsigned char* pbase, int T) { *(LAS f4*)(pbase + (T & 3) * 1024) = v; }
template <bool BF, int Q0, int PART> DI void conv_diag(const v4u (&a)[2][8], const v4u (&bq)[2][4], f4 (&acc)[8], LAS unsigned char* pbase) {
    if constexpr (PART == 0) {
        conv_q<BF, Q0, 0, Q0, Q0 + 4>(a, bq, acc);
        SB();
        conv_q<BF, Q0, 1, Q0 + 1, Q0 + 4>(a, bq, acc);
        SB();
        put_tile(acc[Q0], pbase, Q0);
        asm volatile("" : "+v"(acc[Q0 + 1]), "+v"(acc[Q0 + 2]), "+v"(acc[Q0 + 3]));
        SB();
    } else {
        conv_q<BF, Q0, 2, Q0 + 2, Q0 + 4>(a, bq, acc);
        SB();
        put_tile(acc[Q0 + 1], pbase, Q0 + 1);
        acc[Q0 + 3] = mm<BF>(a[0][0], bq[0][3], acc[Q0 + 3]);
        SB();
        put_tile(acc[Q0 + 2], pbase, Q0 + 2);
        acc[Q0 + 3] = mm<BF>(a[1][0], bq[1][3], acc[Q0 + 3]);
        SB();
    }
}
template <bool BF, int PART> DI void conv_off(const v4u (&a)[2][8], const v4u (&bq)[2][4], f4 (&acc)[8]) {
    conv_q<BF, 0, 2 * PART, 4, 8>(a, bq, acc);
    conv_q<BF, 0, 2 * PART + 1, 4, 8>(a, bq, acc);
    asm volatile("" : "+v"(acc[4]), "+v"(acc[5]), "+v"(acc[6]), "+v"(acc[7]));
    SB();
}
DI u32 pk_bf_s(float a, float b) { u32 r; asm("v_cvt_pk_bf16_f32 %0, %1, %2" : "=v"(r) : "v"(a), "v"(b)); return r; }
DI f4 sp4(float a) { return (f4){a, a, a, a}; }

DI f2 cmul(f2 a, f2 b) {
    f2 r;
    asm("v_pk_mul_f32 %0, %1, %2 op_sel:[0,0] op_sel_hi:[0,1]\n\t"
        "v_pk_fma_f32 %0, %1, %2, %0 op_sel:[1,1,0] op_sel_hi:[1,0,1] neg_lo:[0,1,0]" : "=&v"(r) : "v"(a), "v"(b));
    return r;
}
DI f2 cexp_p(float zr2, float fh, float fl, float p) {
    const float mag = __builtin_amdgcn_exp2f(zr2 * p);
    const float an = fmaf(fl, p, __builtin_amdgcn_fractf(fh * p));
    return (f2){__builtin_amdgcn_cosf(an), __builtin_amdgcn_sinf(an)} * sp2(mag);
}
DI float exp_acc(float x) {
    const float L = 1.44269504088896340736f, Ll = 1.92596299112661746e-08f;
    const float h = x * L;
    const float lo = fmaf(x, Ll, fmaf(x, L, -h));
    const float e = __builtin_amdgcn_exp2f(h);
    return fmaf(e * lo, 0.69314718055994530942f, e);
}

template <bool V> struct BC { static constexpr bool v = V; };
#define STAMP(i) do {} while (0)

__global__ void __launch_bounds__(512, 2)
k1_main(const u16* __restrict__ qin, const u16* __restrict__ kin, const u16* __restrict__ vin, const u16* __restrict__ shk,
        const u16* __restrict__ ssmk, const u16* __restrict__ ssmD, const u16* __restrict__ lar, const u16* __restrict__ aim,
        const u16* __restrict__ cre, const u16* __restrict__ cim, const u16* __restrict__ ldt, const u16* __restrict__ dsk,
        u16* __restrict__ out, unsigned char* ws) {
    extern __shared__ __attribute__((aligned(16))) unsigned char smem[];
    LAS unsigned char* L = (LAS unsigned char*)smem;
    const int t = threadIdx.x, l = t & 63;
    const int w = __builtin_amdgcn_readfirstlane(t >> 6);
    const int wg = (blockIdx.x & 7) * 32 + (blockIdx.x >> 3);
    const int H = (wg >> 1) & 1;
    unsigned long long pc0; asm volatile("s_getpc_b64 %0" : "=s"(pc0));
    auto body = [&](auto hc, auto wc) __attribute__((always_inline)) {
    constexpr bool HH = decltype(hc)::v, WC = decltype(wc)::v;
    asm volatile("" ::: "memory");
    const int rbk = HH * 128 + (wg >> 2) * 2 + (wg & 1);
    constexpr int c1 = WC;
    const int m = t & 255;
    const int rq = w & 3;
    const int n16 = l & 15, g = l >> 4;
    const __amdgpu_buffer_rsrc_t box = __builtin_amdgcn_make_buffer_rsrc(ws, 0, (int)(WS_FLAG + 32768), 0x00020000);
    STAMP(0);
    const v4u sent4 = {SENT, SENT, SENT, SENT};
    const u32 pub_row = (u32)((t >> 2) * 16384 + rbk * 64 + (t & 3) * 16);
    {
        const u32 vo = (u32)l * 16u;
        glds16(kin + (size_t)(8 * rbk + w) * DM, vo, (u32)(L_P + (3 + w) * KV_S));
        glds16(vin + (size_t)(8 * rbk + w) * DM, vo, (u32)(L_P + (11 + w) * KV_S));
        if (!WC && w < 3) {
            if (rbk > 0) glds16(kin + (size_t)(8 * rbk - 3 + w) * DM, vo, (u32)(L_P + w * KV_S));
            else *(LAS v4u*)(L + L_P + w * KV_S + l * 16) = (v4u){0u, 0u, 0u, 0u};
        }
    }
    v4u pf = {0u, 0u, 0u, 0u};
    if (WC && w >= 6) {
        const u32 po = min((u32)(blockIdx.x >> 3) * 2048u + (u32)(t & 127) * 16u, (u32)PF_LEN);
        asm volatile("global_load_dwordx4 %0, %1, %2" : "=v"(pf) : "v"(po), "s"(pc0 & ~127ull) : "memory");
    }
    const int cp = t >> 1, hh = t & 1;
    const v4u sw = *(const v4u*)(shk + cp * 8);
    FENCE();
    const int pch = 2 * wg + ((w >> 1) & 1), pn = t & 127;
    const u32 r_lar = lar[pch * NS + pn], r_aim = aim[pch * NS + pn], r_cre = cre[pch * NS + pn], r_cim = cim[pch * NS + pn];
    const u32 r_cre2 = cre[pch * NS + (pn ^ 64)], r_cim2 = cim[pch * NS + (pn ^ 64)];
    u32 vz; asm volatile("v_mov_b32 %0, 0" : "=v"(vz));
    const u32 r_ldt = ldt[pch + vz];
    const u32 dA2 = *(const u32*)(ssmD + 2 * wg + vz), dB2 = *(const u32*)(dsk + 2 * wg + vz);
    FENCE();
    const int chg = 2 * wg + c1;
    const v4u fcur = *(const v4u*)(ssmk + (size_t)chg * SEQ + 8 * m);
    v2u fprv = *(const v2u*)(ssmk + (size_t)chg * SEQ + (m > 0 ? 8 * m - 4 : 0));
    FENCE();
    const u32 ini_y = (u32)((rbk >> 1) * 16384 + w * 2048 + (rbk & 1) * 1024 + l * 16);
    const u32 ini_u = (u32)((wg >> 1) * 16384 + w * 2048 + (wg & 1) * 1024 + l * 16);
    box_put(sent4, box, ini_y, 1 * MB2);
    FENCE();
    const u32 c_s = now(), r_s = rtime();
    const int row = 8 * rbk + w;
    v2u nonce;
    {
        const unsigned long long id = amd_dispatch_id(), dp = (unsigned long long)__builtin_amdgcn_dispatch_ptr();
        const u32 a = (u32)id * 0x9E3779B1u + (u32)(dp >> 32);
        const u32 b = ((u32)dp ^ ((u32)(id >> 32) * 0x85EBCA77u)) & ~1u;
        nonce = (v2u){a, b | (~a & 1u)};
    }
    const u32 flag_w = t < 8 ? WS_FLAG + (u32)(t * 256 + rbk) * 16 : 0x7FFFFFF0u;
    FENCE();
    if (m < 30) *(LAS v4u*)(L + L_U + c1 * U_S + 16 * m) = (v4u){0u, 0u, 0u, 0u};
    if (m < 8) *(LAS v4u*)(L + L_KC + (c1 * 4 + (m >> 1)) * KC_S + (m & 1) * 16) = (v4u){0u, 0u, 0u, 0u};

    LAS const unsigned char* abase = L + L_KC + (c1 * 4 + (n16 >> 2)) * KC_S + 56 - 16 * g + 8 * (n16 & 3) + 128 * rq;
    LAS const unsigned char* bbase = L + L_U + c1 * U_S + 384 + 32 * n16 + 16 * g - 128 * rq;
    LAS unsigned char* pbase = L + L_P + (rq * 2 + c1) * 4096 + (g * 16 + ((n16 + 4 * g) & 15)) * 16;
    const int eg = l & 3, en = l >> 2;
    LAS const unsigned char* prd = L + L_P + rq * 1024 + (eg * 16 + ((en + 4 * eg) & 15)) * 16;
    LAS const unsigned char* urd = L + L_U + 480 + w * 512 + l * 8;
    LAS unsigned char* ust = L + L_U + (t & 1) * U_S + 480 + (t >> 1) * 16;
    LAS unsigned char* kcw = L + L_KC + c1 * 4 * KC_S + 32 + 16 * m;
    LAS u32* ti = (LAS u32*)(L + L_TI) + (4 * (t & 1)) * 260 + (t >> 1);
    LAS const unsigned char* tr = L + L_TO + ((l & 15) >> 2) * 1088 + (w * 64 + (l >> 4) * 16 + (l & 3) * 4) * 2;
    const u32 pub_col = (u32)((w * 16 + en) * 16384 + wg * 64 + eg * 16);
    const u32 get_p = (u32)((t >> 1) * 64 + (t & 1) * 16);
    const unsigned char* gu = ws + (wg >> 1) * 16384 + (wg & 1) * 32;
    const unsigned char* gy = ws + (rbk >> 1) * 16384 + (rbk & 1) * 32;
    const unsigned char* flag_b = ws + WS_FLAG + (wg >> 5) * 4096;
    const u32 flag_o = (u32)(t >> 1) * 16;

    {
        f2 w0 = {lo2f(sw.x), lo2f(sw.z)}, w1 = {hi2f(sw.x), hi2f(sw.z)}, w2 = {lo2f(sw.y), lo2f(sw.w)}, w3 = {hi2f(sw.y), hi2f(sw.w)};
        asm volatile("" : "+v"(w0), "+v"(w1), "+v"(w2), "+v"(w3));
        asm volatile("" :: "v"(pf));
        lds_bar();
        STAMP(1);
        LAS const unsigned char* kp = L + L_P + 4 * hh * KV_S + cp * 4;
        u32 kr[7], vr[4];
#pragma unroll
        for (int i = 0; i < 7; ++i) kr[i] = *(LAS const u32*)(kp + i * KV_S);
#pragma unroll
        for (int i = 0; i < 4; ++i) vr[i] = *(LAS const u32*)(kp + (11 + i) * KV_S);
        f2 kf[7], kv[4];
#pragma unroll
        for (int i = 0; i < 7; ++i) kf[i] = up2(kr[i]);
#pragma unroll
        for (int r = 0; r < 4; ++r) kv[r] = fma2(kf[r + 3], w0, fma2(kf[r + 2], w1, fma2(kf[r + 1], w2, kf[r] * w3))) * up2(vr[r]);
        const u32 p00 = pk_bf(kv[0].x, kv[1].x), p01 = pk_bf(kv[2].x, kv[3].x);
        const u32 p10 = pk_bf(kv[0].y, kv[1].y), p11 = pk_bf(kv[2].y, kv[3].y);
        const u32 s0 = hh ? p00 : p10, s1 = hh ? p01 : p11;
        const u32 r0 = dppu<0xB1>(s0), r1 = dppu<0xB1>(s1);
        v4u o = hh ? (v4u){r0, r1, p10, p11} : (v4u){p00, p01, r0, r1};
        asm volatile("" : "+v"(o));
            box_put(o, box, pub_row, 0);
        FENCE();
    }
    STAMP(2); STAMP(3);
    f2 C, e128, ea, eb;
    {
        const float dt = rb(exp_acc(bf2f(r_ldt)));
        const float are = rb(exp_acc(bf2f(r_lar)));
        const float zr2 = -(are * dt) * 1.44269504088896340736f;
        const float zi = bf2f(r_aim) * dt;
        const float chi = 0.15915494309189533577f, clo = 6.420638326565253e-09f;
        const float fh = zi * chi;
        const float fl2 = fmaf(zi, clo, fmaf(zi, chi, -fh));
        const u32 am = max(max(r_cre & 0x7FFFu, r_cim & 0x7FFFu), max(r_cre2 & 0x7FFFu, r_cim2 & 0x7FFFu));
        const float mx = bf2f(wave_maxu(am)) * dt;
        int ec = __builtin_amdgcn_frexp_expf(mx);
        ec = ec < -100 ? -100 : ec;
        const float sA = __builtin_amdgcn_ldexpf(1.0f, 13 - ec);
        C = (f2){bf2f(r_cre), -bf2f(r_cim)} * sp2(dt) * sp2(sA);
        e128 = cexp_p(zr2, -fh, -fl2, 128.f);
        ea = cexp_p(zr2, -fh, -fl2, WC ? 1.f : 4.f);
        eb = cexp_p(zr2, -fh, -fl2, WC ? 2.f : 512.f);
        if (l == 0) *(LAS int*)(L + L_KCMP + ((w >> 1) & 1) * KCMP_S + 2824) = ec;
        asm volatile("" : "+v"(C), "+v"(e128), "+v"(ea), "+v"(eb));
        SB();
    }
    STAMP(4);
    {
        if (m == 0) fprv = (v2u){0u, 0u};
        const u32 d[6] = {fprv.x, fprv.y, fcur.x, fcur.y, fcur.z, fcur.w};
        write_copies(kcw, d);
    }
    FENCE();
    v4u fl, q16;
    v4u af[2][8];
    v4u pc;
    {
        LAS unsigned char* tb = L + L_TB + ((w >> 1) & 1) * TB_S + pn * 4;
#define PUT(rowi, v) *(LAS u32*)(tb + (rowi) * TB_R) = pk_h((v).x, (v).y)
#define MIDDLE() do { \
        SB(); \
        vm_wait();                 \
        lds_bar(); \
        if (!WC && w == 0) box_put((v4u){nonce.x, nonce.y, rtime(), 0u}, box, flag_w, 0); \
        tfl = now(); \
        q16 = *(const v4u*)(qin + (size_t)row * DM + 8 * l); \
        FENCE(); \
        STAMP(5); \
        load_afrags(af, abase); } while (0)
#define MIDDLE2() do { \
        SB(); \
        wait_until(tfl, D_FL + (31 - (blockIdx.x >> 3)) * FL_K); \
        fl = box_req(flag_b, flag_o); \
        FENCE(); } while (0)
        u32 tfl;
        if constexpr (!WC) {
            f2 G = cmul(C, eb);
            f2 e8 = cmul(ea, ea); e8.y = -e8.y;
            f2 B = {16384.f, 0.f};
#pragma unroll
            for (int r = 0; r < 16; ++r) {
                const int h = r + 4;
                if (r == MID_A) { asm volatile("" : "+v"(G), "+v"(B), "+v"(e8)); MIDDLE(); }
                if (r == MID_B) { asm volatile("" : "+v"(G), "+v"(B), "+v"(e8)); MIDDLE2(); }
                PUT(41 + r, B); if (r < 15) B = cmul(B, e8);
                if (h < 8) { PUT(24 + 2 * (h - 4), G); const f2 G4 = cmul(G, ea); PUT(25 + 2 * (h - 4), G4); }
                else if (h <= 16) PUT(24 + h, G);
                if (h < 16) G = cmul(G, e128);
            }
        } else {
            f2 e[8];
            e[1] = ea; e[2] = eb; e[3] = cmul(e[2], e[1]); e[4] = cmul(e[2], e[2]);
            e[5] = cmul(e[4], e[1]); e[6] = cmul(e[4], e[2]); e[7] = cmul(e[4], e[3]);
            f2 G = C;
#pragma unroll
            for (int h = 0; h < 4; ++h) {
                if (h == MID_C) { asm volatile("" : "+v"(G)); MIDDLE(); }
                if (h == MID_D) { asm volatile("" : "+v"(G)); MIDDLE2(); }
                if (h < 2) {
                    PUT(8 * h, G);
#pragma unroll
                    for (int a = 1; a < 8; ++a) { const f2 v = cmul(G, e[a]); PUT(8 * h + a, v); }
                } else {
                    PUT(16 + 4 * (h - 2), G);
#pragma unroll
                    for (int a = 1; a < 4; ++a) { const f2 v = cmul(G, e[2 * a]); PUT(16 + 4 * (h - 2) + a, v); }
                }
                if (h < 3) G = cmul(G, e128);
            }
        }
#undef PUT
    }
    STAMP(6);

    f4 acc[8];
    v4u bq[2][4];
    v4u py, py2;
#define EPILOGUE(BOXI, S0, S1) do { \
        f4 pp[8]; \
        _Pragma("unroll") for (int i = 0; i < 8; ++i) pp[i] = *(LAS const f4*)(prd + i * 4096); \
        const f4 y0 = __builtin_elementwise_fma((pp[0] + pp[2]) + (pp[4] + pp[6]), sp4(S0), e0); \
        const f4 y1 = __builtin_elementwise_fma((pp[1] + pp[3]) + (pp[5] + pp[7]), sp4(S1), e1); \
        v4u o; \
        _Pragma("unroll") for (int r = 0; r < 4; ++r) o[r] = clampS(pk_bf_s(y0[r], y1[r])); \
        box_put(o, box, pub_col, (BOXI) * MB2); \
        FENCE(); \
        __builtin_amdgcn_s_setprio(0); } while (0)
#define ZERO_ACC() do { _Pragma("unroll") for (int i = 0; i < 8; ++i) acc[i] = (f4){0.f, 0.f, 0.f, 0.f}; } while (0)
#define PUT_TI(p, o) do { ti[(o)] = p.x; ti[260 + (o)] = p.y; ti[520 + (o)] = p.z; ti[780 + (o)] = p.w; } while (0)
#define KGEN_MM(ch, mt, bfr) do { \
        LAS const unsigned char* ta = L + L_TB + (ch) * TB_S + (16 * (mt) + n16) * TB_R + 16 * g; \
        v4u afr[8]; \
        _Pragma("unroll") for (int s = 0; s < 8; ++s) afr[s] = *(LAS const v4u*)(ta + 64 * s); \
        f4 d0 = {0.f, 0.f, 0.f, 0.f}, d1 = {0.f, 0.f, 0.f, 0.f}; \
        _Pragma("unroll") for (int s = 0; s < 8; s += 2) { d0 = mm<false>(afr[s], bfr[s], d0); d1 = mm<false>(afr[s + 1], bfr[s + 1], d1); } \
        const f4 dacc = d0 + d1; \
        const int pb = 8 * n16 + ((mt) == 0 ? 4 * (g & 1) + 128 * (g >> 1) : (mt) == 1 ? (g < 2 ? 256 + 128 * g : 256 * g) : 1024 + 512 * g); \
        const int s1 = (mt) == 0 ? 1 : (mt) == 1 ? (g < 2 ? 2 : 4) : 128, s2 = (mt) == 0 ? 2 : (mt) == 1 ? (g < 2 ? 4 : 128) : 256; \
        _Pragma("unroll") for (int i = 0; i < 4; ++i) { \
            int p = pb + (i & 1) * s1 + (i >> 1) * s2; \
            p = p > 2048 ? 2064 : p; \
            u32 ad = __builtin_bit_cast(u32, (float)p) >> 15; \
            if (i == 0) ad = ad > 0x7EFEu ? ad : 0x7EFEu; \
            *(LAS _Float16*)(L + L_KCMP + (ch) * KCMP_S - 0x7EFE + ad) = (_Float16)(dacc[i] * 1.9073486328125e-06f);     \
        } } while (0)
#define EXPAND() do { \
        u32 d[6]; \
        const float f0 = (float)(8 * m - 4); \
        const u32 kb = (u32)(L_KCMP + c1 * KCMP_S - 2 * 0x3F7F); \
        _Pragma("unroll") for (int i = 0; i < 6; ++i) { \
            float fa = f0 + (float)(2 * i), fb = f0 + (float)(2 * i + 1); \
            if (i < 3) fa = fmaxf(fa, 0.99609375f); \
            if (i < 2) fb = fmaxf(fb, 0.99609375f); \
            const u32 cd = pk_bf(fa, fb); \
            u32 a0, a1; \
            asm("v_mad_u32_u16 %0, %2, 2, %3\n\tv_mad_u32_u16 %1, %2, 2, %3 op_sel:[1,0,0,0]" : "=&v"(a0), "=v"(a1) : "v"(cd), "s"(kb)); \
            us2 v; v.x = *(LAS const u16*)(L + a0); v.y = *(LAS const u16*)(L + a1); \
            d[i] = __builtin_bit_cast(u32, v); \
        } \
        if (m == 0) { d[0] = 0u; d[1] = 0u; } \
        write_copies(kcw, d); \
        SB(); } while (0)
    {
        asm volatile("s_waitcnt vmcnt(0)" : "+v"(fl) :: "memory");
#pragma unroll 1
        for (u32 sp = 0; sp < (1u << 16); ++sp) {
            const unsigned long long bad = __builtin_amdgcn_ballot_w64(fl.x != nonce.x || fl.y != nonce.y);
            if (!bad) break;
            unsigned long long sv;
            asm volatile("s_sleep 1\n\ts_mov_b64 %1, exec\n\ts_mov_b64 exec, %4\n\tglobal_load_dwordx4 %0, %2, %3 sc1\n\ts_mov_b64 exec, %1\n\ts_waitcnt vmcnt(0)"
                         : "+v"(fl), "=&s"(sv) : "v"(flag_o), "s"(flag_b), "s"(bad) : "memory");
        }
        asm volatile("" : "+v"(q16));
        pc = box_req(gu, get_p);
        FENCE();
        STAMP(7);
    }
    lds_bar();
    if (w < 6) {
        const int ch = w >= 3;
        LAS const unsigned char* tbb = L + L_TB + ch * TB_S + (41 + n16) * TB_R + 16 * g;
        v4u bfr[8];
#pragma unroll
        for (int s = 0; s < 8; ++s) bfr[s] = *(LAS const v4u*)(tbb + 64 * s);
        const int mt = w - 3 * ch;
        KGEN_MM(ch, mt, bfr);
    }
    SB();
    {
        const u32 c_n = now(), r_n = rtime();
        const float cpt = (float)(c_n - c_s) * __builtin_amdgcn_rcpf((float)(r_n - r_s));
        const u32 epw = c_n + (u32)(int)((float)wave_maxi((int)(fl.z - r_n)) * cpt);
        if (l == 0) *(LAS u32*)(L + L_EP + 4 * w) = epw;
    }
    SB();
    float rs0, nb0;
    u32 ep;
    {
        STAMP(8);
        asm volatile("s_waitcnt vmcnt(0)" : "+v"(pc) :: "memory");
        *(LAS v4u*)ust = pc;
        box_put(sent4, box, ini_u, 2 * MB2);
        box_put(sent4, box, ini_y, 3 * MB2);
        lds_bar();
        STAMP(9);
        load_b<0>(bq, bbase);
        const v2u i0 = *(LAS const v2u*)(urd), i1 = *(LAS const v2u*)(urd + U_S);
        ZERO_ACC();
        SB();
        conv_diag<true, 0, 0>(af, bq, acc, pbase);
        conv_diag<true, 0, 1>(af, bq, acc, pbase);
        f4 e0 = (f4){lo2f(i0.x), hi2f(i0.x), lo2f(i0.y), hi2f(i0.y)} * sp4(lo2f(dA2));
        f4 e1 = (f4){lo2f(i1.x), hi2f(i1.x), lo2f(i1.y), hi2f(i1.y)} * sp4(hi2f(dA2));
        asm volatile("" : "+v"(e0), "+v"(e1));
        SB();
        put_tile(acc[3], pbase, 3);
        if constexpr (!WC) {
            __builtin_amdgcn_s_setprio(3);
            asm volatile("s_waitcnt vmcnt(1)" ::: "memory");
            lds_bar();
            EPILOGUE(1, 1.0f, 1.0f);
        } else {
            lds_bar();
        }
        STAMP(10);
        {
            const v4u ea_ = *(LAS const v4u*)(L + L_EP), eb_ = *(LAS const v4u*)(L + L_EP + 16);
            const u32 r = ea_.x;
            const int d = max(max(max((int)(ea_.y - r), (int)(ea_.z - r)), max((int)(ea_.w - r), (int)(eb_.x - r))),
                              max(max((int)(eb_.y - r), (int)(eb_.z - r)), max((int)(eb_.w - r), 0)));
            ep = __builtin_amdgcn_readfirstlane(r + (u32)d);
        }
        v4u b2[2][4];
        conv_off<true, 0>(af, bq, acc);
        load_b<4>(b2, bbase);
        conv_off<true, 1>(af, bq, acc);
        lds_bar();
        STAMP(11);
        conv_diag<true, 4, 0>(af, b2, acc, pbase);
        if constexpr (!HH && !WC) { wait_until(ep, S_YA0); py = box_req(gy + 1 * MB2, get_p); py2 = box_req(gy + 1 * MB2 + 8192, get_p); SB(); }
        conv_diag<true, 4, 1>(af, b2, acc, pbase);
        put_tile(acc[7], pbase, 7);
        if constexpr (WC) { __builtin_amdgcn_s_setprio(3); asm volatile("s_waitcnt vmcnt(1)" ::: "memory"); }
        lds_bar();
        if constexpr (WC) EPILOGUE(1, 1.0f, 1.0f);
        STAMP(12);
        box_put(sent4, box, ini_u, 4 * MB2);
        FENCE();
        const f2 xq[4] = {up2(q16.x), up2(q16.y), up2(q16.z), up2(q16.w)};
        ln_stats(xq, 1.0f, rs0, nb0);
    }
    const float sc0 = __builtin_amdgcn_ldexpf(1.0f / USCALE, *(LAS const int*)(L + L_KCMP + 2824) - 7);
    const float sc1 = __builtin_amdgcn_ldexpf(1.0f / USCALE, *(LAS const int*)(L + L_KCMP + KCMP_S + 2824) - 7);

    v4u yAp;
    f2 x[4] = {up2(q16.x), up2(q16.y), up2(q16.z), up2(q16.w)};
#pragma unroll
    for (int it = 0; it < 3; ++it) {
        {
            f2 gate[4];
            if (it == 0) {
#pragma unroll
                for (int i = 0; i < 4; ++i) gate[i] = fma2(x[i], sp2(rs0), sp2(nb0));
            } else if (it == 1) { gate[0] = up2(yAp.x); gate[1] = up2(yAp.y); gate[2] = up2(yAp.z); gate[3] = up2(yAp.w); }
            if (it < 2) asm volatile("" : "+v"(gate[0]), "+v"(gate[1]), "+v"(gate[2]), "+v"(gate[3]));
            if constexpr (HH) {
                if (it == 0) { EXPAND(); lds_bar(); }
                wait_until(ep, it == 0 ? S_YA1 : it == 1 ? S_Y10 : S_Y11);
                py = box_req(gy + (1 + 2 * it) * MB2, get_p);
                if (it == 0) { SB(); load_afrags(af, abase); SB(); }
                py = box_fin(0, py, gy + (1 + 2 * it) * MB2, get_p, 1 + 4 * it);
                PUT_TI(py, 0);
                if (it < 2 && !WC) { wait_until(ep, it ? S_U01 : S_U00); pc = box_req(gu + (2 + 2 * it) * MB2, get_p); }
            } else if constexpr (!WC) {
                py = box_fin(it == 0 ? 2 : 1, py, gy + (1 + 2 * it) * MB2, get_p, 4 * it);
                PUT_TI(py, 0);
                py2 = box_fin(it == 0 ? 1 : 0, py2, gy + (1 + 2 * it) * MB2 + 8192, get_p, 4 * it);
                PUT_TI(py2, 128);
            } else {
                asm volatile("s_waitcnt vmcnt(%0)" :: "i"(it == 0 ? 2 : 1) : "memory");
            }
            lds_bar();
            STAMP(13 + 7 * it);
            const v4u yv = *(LAS const v4u*)(L + L_TI + w * 1040 + l * 16);
            const f2 yc[4] = {up2(yv.x), up2(yv.y), up2(yv.z), up2(yv.w)};
            float rs, nb;
            if (it == 0) {
#pragma unroll
                for (int i = 0; i < 4; ++i) x[i] = fma2(gate[i], yc[i], x[i]);
                yAp = yv;
            } else {
#pragma unroll
                for (int i = 0; i < 4; ++i) x[i] += yc[i];
            }
            if (it == 2) {
                *(v4u*)(out + (size_t)row * DM + 8 * l) = (v4u){pk_bf(x[0].x, x[0].y), pk_bf(x[1].x, x[1].y), pk_bf(x[2].x, x[2].y), pk_bf(x[3].x, x[3].y)};
                STAMP(14 + 7 * it);
                break;
            }
            if (it == 1) {
                ln_stats(x, 1.0f, rs, nb);
#pragma unroll
                for (int i = 0; i < 4; ++i) x[i] = fma2(fma2(x[i], sp2(rs), sp2(nb)), gate[i], x[i]);
            }
            ln_stats(x, USCALE, rs, nb);
            v4u uo;
#pragma unroll
            for (int i = 0; i < 4; ++i) { const f2 u = fma2(x[i], sp2(rs), sp2(nb)); uo[i] = pk_h(u.x, u.y); }
            *(LAS v4u*)(L + L_TO + w * 1088 + l * 16) = uo;
            lds_bar();
            const v2u lo2 = tr_read(tr), hi2 = tr_read(tr + 4 * 1088);
            box_put((v4u){clampS(lo2.x), clampS(lo2.y), clampS(hi2.x), clampS(hi2.y)}, box, pub_row, (2 + 2 * it) * MB2);
            FENCE();
            STAMP(14 + 7 * it);
        }
        {
            if constexpr (HH) {
                if constexpr (!WC) { pc = box_fin(1, pc, gu + (2 + 2 * it) * MB2, get_p, 2 + 4 * it); *(LAS v4u*)ust = pc; }
            } else {
                if (it == 0) { EXPAND(); lds_bar(); }
                if constexpr (!WC) { wait_until(ep, it ? S_U01 : S_U00); pc = box_req(gu + (2 + 2 * it) * MB2, get_p); }
                if (it == 0) { SB(); load_afrags(af, abase); SB(); }
                if constexpr (!WC) { pc = box_fin(0, pc, gu + (2 + 2 * it) * MB2, get_p, 2 + 4 * it); *(LAS v4u*)ust = pc; }
            }
            lds_bar();
            STAMP(15 + 7 * it);
            load_b<0>(bq, bbase);
            const float d0 = lo2f(dB2) * (1.0f / USCALE), d1 = hi2f(dB2) * (1.0f / USCALE);
#define SKIP_LOAD() const v2u i0 = *(LAS const v2u*)(urd), i1 = *(LAS const v2u*)(urd + U_S)
#define SKIP_TERMS() \
            const h2 h00 = as_h2(i0.x), h01 = as_h2(i0.y), h10 = as_h2(i1.x), h11 = as_h2(i1.y); \
            f4 e0 = (f4){(float)h00.x, (float)h00.y, (float)h01.x, (float)h01.y} * sp4(d0); \
            f4 e1 = (f4){(float)h10.x, (float)h10.y, (float)h11.x, (float)h11.y} * sp4(d1); \
            asm volatile("" : "+v"(e0), "+v"(e1)); SB()
            ZERO_ACC();
            if constexpr (!WC) {
                SKIP_LOAD();
                SB();
                conv_diag<false, 0, 0>(af, bq, acc, pbase);
                conv_diag<false, 0, 1>(af, bq, acc, pbase);
                SKIP_TERMS();
                put_tile(acc[3], pbase, 3);
                __builtin_amdgcn_s_setprio(3);
                lds_bar();
                STAMP(16 + 7 * it);
                EPILOGUE(3 + 2 * it, sc0, sc1);
            } else {
                SB();
                conv_diag<false, 0, 0>(af, bq, acc, pbase);
                conv_diag<false, 0, 1>(af, bq, acc, pbase);
                put_tile(acc[3], pbase, 3);
                if constexpr (HH) { wait_until(ep, it ? S_U11 : S_U10); pc = box_req(gu + (2 + 2 * it) * MB2, get_p); }
                lds_bar();
                STAMP(16 + 7 * it);
            }
            if (it == 0 && !(HH && WC)) {
                box_put(sent4, box, ini_y, 5 * MB2);
                if (!WC && w == 0) __builtin_amdgcn_raw_buffer_store_b64(~nonce, box, flag_w, 0, 16);
                FENCE();
            }
            conv_off<false, 0>(af, bq, acc);
            if constexpr (WC && !HH) { wait_until(ep, it ? S_U11 : S_U10); pc = box_req(gu + (2 + 2 * it) * MB2, get_p); SB(); }
            conv_off<false, 1>(af, bq, acc);
            if constexpr (WC) {
                pc = box_fin(0, pc, gu + (2 + 2 * it) * MB2, get_p, 3 + 4 * it);
                *(LAS v4u*)ust = pc;
                if (it == 0 && HH) {
                    box_put(sent4, box, ini_y, 5 * MB2);
                    FENCE();
                }
            }
            STAMP(17 + 7 * it);
            lds_bar();
            STAMP(18 + 7 * it);
            load_b<4>(bq, bbase);
            if constexpr (WC) {
                SKIP_LOAD();
                SB();
                conv_diag<false, 4, 0>(af, bq, acc, pbase);
                conv_diag<false, 4, 1>(af, bq, acc, pbase);
                SKIP_TERMS();
                put_tile(acc[7], pbase, 7);
                __builtin_amdgcn_s_setprio(3);
                lds_bar();
                EPILOGUE(3 + 2 * it, sc0, sc1);
            } else {
                conv_diag<false, 4, 0>(af, bq, acc, pbase);
                if constexpr (!HH) {
                    wait_until(ep, it ? S_Y01 : S_Y00);
                    py = box_req(gy + (3 + 2 * it) * MB2, get_p); py2 = box_req(gy + (3 + 2 * it) * MB2 + 8192, get_p); SB();
                }
                conv_diag<false, 4, 1>(af, bq, acc, pbase);
                put_tile(acc[7], pbase, 7);
                lds_bar();
            }
            STAMP(19 + 7 * it);
        }
    }
    };
    if (H) { if (w < 4) body(BC<true>{}, BC<false>{}); else body(BC<true>{}, BC<true>{}); }
    else   { if (w < 4) body(BC<false>{}, BC<false>{}); else body(BC<false>{}, BC<true>{}); }
}

extern "C" void kernel_launch(void* const* d_in, const int* in_sizes, int n_in, void* d_out, int out_size, void* d_ws, size_t ws_size,
                              hipStream_t stream) {
    (void)hipFuncSetAttribute((const void*)k1_main, hipFuncAttributeMaxDynamicSharedMemorySize, L_END);
    k1_main<<<256, 512, L_END, stream>>>((const u16*)d_in[0], (const u16*)d_in[1], (const u16*)d_in[2], (const u16*)d_in[3], (const u16*)d_in[4],
                                         (const u16*)d_in[5], (const u16*)d_in[7], (const u16*)d_in[8], (const u16*)d_in[9], (const u16*)d_in[10],
                                         (const u16*)d_in[11], (const u16*)d_in[12], (u16*)d_out, (unsigned char*)d_ws);
}
```

```cpp
#include <hip/hip_runtime.h>

typedef unsigned short u16;
typedef unsigned u32;
typedef u32 v4u __attribute__((ext_vector_type(4)));
typedef u32 v2u __attribute__((ext_vector_type(2)));
typedef float f4 __attribute__((ext_vector_type(4)));
typedef float f2 __attribute__((ext_vector_type(2)));
typedef __bf16 bf8 __attribute__((ext_vector_type(8)));
typedef __bf16 bf2 __attribute__((ext_vector_type(2)));
typedef _Float16 h8 __attribute__((ext_vector_type(8)));
typedef _Float16 h2 __attribute__((ext_vector_type(2)));
typedef short s4 __attribute__((ext_vector_type(4)));
typedef unsigned short us2 __attribute__((ext_vector_type(2)));
#define LAS __attribute__((address_space(3)))
#define DI __device__ __forceinline__
extern "C" __device__ unsigned long long amd_dispatch_id() __asm("llvm.amdgcn.dispatch.id");

constexpr int SEQ = 2048, DM = 512, NS = 128;
constexpr u32 SENT = 0xFFFFFFFFu;
constexpr u32 MB2 = 2u * 1024u * 1024u;
constexpr u32 WS_FLAG = 6 * MB2;
constexpr size_t WS_STAMP = 8 * (size_t)MB2;
constexpr float USCALE = 16.0f;
#define D_FL 700
#define MID_A 0
#define MID_B 8
#define MID_C 0
#define MID_D 2
#define FL_K 25
#define DLY 0
#define INI_EARLY 1
#define S_YA0 6500
#define S_YA1 9000
#define S_U00 11000
#define S_U10 13500
#define S_Y00 15500
#define S_Y10 18000
#define S_U01 20500
#define S_U11 23000
#define S_Y01 25000
#define S_Y11 27500

constexpr int KC_S = 4160;
constexpr int L_KC = 0;
constexpr int U_S = 4672;
constexpr int L_U = L_KC + 2 * 4 * KC_S;
constexpr int L_TI = L_U + 2 * U_S;
constexpr int L_TO = L_TI + 8 * 1040;
constexpr int KCMP_S = 2832;
constexpr int L_KCMP = L_TO + 8 * 1088;
constexpr int TB_R = 544, TB_S = 57 * TB_R;
constexpr int L_TB = L_KCMP + 2 * KCMP_S;
constexpr int L_P = L_TB + 2 * TB_S;
constexpr int KV_S = 1056;
constexpr int L_EP = L_P + 32768;
constexpr int L_END = L_EP + 64;

DI float bf2f(u32 h) { return __builtin_bit_cast(float, h << 16); }
DI float lo2f(u32 d) { return __builtin_bit_cast(float, d << 16); }
DI float hi2f(u32 d) { return __builtin_bit_cast(float, d & 0xFFFF0000u); }
DI f2 up2(u32 d) { return (f2){lo2f(d), hi2f(d)}; }
DI float rb(float x) { f2 t = {0.f, x}; return __builtin_bit_cast(float, __builtin_convertvector(t, bf2)); }
DI u32 pk_bf(float a, float b) { f2 t = {a, b}; return __builtin_bit_cast(u32, __builtin_convertvector(t, bf2)); }
DI u32 pk_h(float a, float b) { f2 t = {a, b}; return __builtin_bit_cast(u32, __builtin_convertvector(t, h2)); }
DI h2 as_h2(u32 d) { return __builtin_bit_cast(h2, d); }
DI u32 clampS(u32 d) { return d < 0xFFFFFFFEu ? d : 0xFFFFFFFEu; }
DI f2 fma2(f2 a, f2 b, f2 c) { return __builtin_elementwise_fma(a, b, c); }
DI f2 sp2(float a) { return (f2){a, a}; }
template <int CTRL, int RM = 0xf> DI float dppf(float v) {
    return __builtin_bit_cast(float, __builtin_amdgcn_update_dpp(0, __builtin_bit_cast(int, v), CTRL, RM, 0xf, true));
}
DI float rdl(float v, int l) { return __builtin_bit_cast(float, __builtin_amdgcn_readlane(__builtin_bit_cast(int, v), l)); }
DI void wave_sum2(float& a, float& b) {
    const auto sw = __builtin_amdgcn_permlane32_swap(__builtin_bit_cast(u32, a), __builtin_bit_cast(u32, b), false, false);
    float r = __builtin_bit_cast(float, (u32)sw[0]) + __builtin_bit_cast(float, (u32)sw[1]);
    r += dppf<0xB1>(r); r += dppf<0x4E>(r); r += dppf<0x141>(r); r += dppf<0x140>(r);
    asm volatile("s_nop 1\n\tv_add_f32_dpp %0, %0, %0 row_bcast:15 row_mask:0xa bank_mask:0xf\n\ts_nop 0" : "+v"(r));
    a = rdl(r, 31); b = rdl(r, 63);
}
template <int CTRL, int RM = 0xf> DI u32 dppu(u32 v) { return (u32)__builtin_amdgcn_update_dpp(0, (int)v, CTRL, RM, 0xf, true); }
DI u32 wave_maxu(u32 v) {
    v = max(v, dppu<0xB1>(v)); v = max(v, dppu<0x4E>(v)); v = max(v, dppu<0x141>(v)); v = max(v, dppu<0x140>(v));
    return max(max((u32)__builtin_amdgcn_readlane(v, 0), (u32)__builtin_amdgcn_readlane(v, 16)),
               max((u32)__builtin_amdgcn_readlane(v, 32), (u32)__builtin_amdgcn_readlane(v, 48)));
}
DI void ln_stats(const f2* x, float scale, float& rs, float& nb) {
    const float c = rdl(x[0].x, 0);
    f2 d[4];
#pragma unroll
    for (int i = 0; i < 4; ++i) d[i] = x[i] - sp2(c);
    const f2 s2 = (d[0] + d[1]) + (d[2] + d[3]);
    const f2 q2 = fma2(d[3], d[3], fma2(d[2], d[2], fma2(d[1], d[1], d[0] * d[0])));
    float s = s2.x + s2.y, q = q2.x + q2.y;
    wave_sum2(s, q);
    const float md = s * (1.0f / DM);
    const float var = fmaxf(fmaf(-md, md, q * (1.0f / DM)), 0.f);
    const float mb = rb(c + md);
    const float sd = rb(__builtin_amdgcn_sqrtf(rb(rb(var) + 1e-5f)));
    rs = __builtin_amdgcn_rcpf(sd) * scale;
    nb = -mb * rs;
}

DI bool has_sent(v4u v) { const u32 a = v.x > v.y ? v.x : v.y, b = v.z > v.w ? v.z : v.w; return (a > b ? a : b) == SENT; }
#define SB() __builtin_amdgcn_sched_barrier(0)
#define FENCE() do { asm volatile("" ::: "memory"); SB(); } while (0)
DI u32 now() { unsigned long long t_; asm volatile("s_memtime %0\n\ts_waitcnt lgkmcnt(0)" : "=s"(t_) :: "memory"); return (u32)t_; }
DI void wait_until(u32 t0, u32 d) {
#pragma unroll 1
    for (int i = 0; i < 512; ++i) if ((int)(now() - t0 - d) >= 0) break;
}
DI u32 rtime() { unsigned long long t_; asm volatile("s_memrealtime %0\n\ts_waitcnt lgkmcnt(0)" : "=s"(t_) :: "memory"); return (u32)t_; }
DI int wave_maxi(int v) {
    v = max(v, (int)dppu<0xB1>(v)); v = max(v, (int)dppu<0x4E>(v)); v = max(v, (int)dppu<0x141>(v)); v = max(v, (int)dppu<0x140>(v));
    return max(max(__builtin_amdgcn_readlane(v, 0), __builtin_amdgcn_readlane(v, 16)), max(__builtin_amdgcn_readlane(v, 32), __builtin_amdgcn_readlane(v, 48)));
}
DI v4u box_req(const unsigned char* base, u32 off) {
    v4u v; asm volatile("global_load_dwordx4 %0, %1, %2 sc1" : "=v"(v) : "v"(off), "s"(base) : "memory"); return v;
}
#define box_fin(N, v, base, off, id) box_fin_(N, v, base, off)
DI v4u box_fin_(int N, v4u v, const unsigned char* base, u32 off) {
    asm volatile("s_waitcnt vmcnt(%1)" : "+v"(v) : "i"(N) : "memory");
#pragma unroll 1
    for (u32 sp = 0; sp < (1u << 16); ++sp) {
        const unsigned long long bad = __builtin_amdgcn_ballot_w64(has_sent(v));
        if (!bad) break;
        unsigned long long sv;
        asm volatile("s_sleep 2\n\ts_mov_b64 %1, exec\n\ts_mov_b64 exec, %4\n\tglobal_load_dwordx4 %0, %2, %3 sc1\n\ts_mov_b64 exec, %1\n\ts_waitcnt vmcnt(0)"
                     : "+v"(v), "=&s"(sv) : "v"(off), "s"(base), "s"(bad) : "memory");
    }
    return v;
}
DI void box_put(v4u v, __amdgpu_buffer_rsrc_t r, u32 off, u32 soff) {
    asm volatile("" : "+v"(v));
    __builtin_amdgcn_raw_buffer_store_b128(v, r, off, soff, 16);
    asm volatile("s_nop 1" :: "v"(v) : "memory");
}
DI void glds16(const void* base, u32 off, u32 lds) {
    asm volatile("s_mov_b32 m0, %2\n\ts_nop 0\n\tglobal_load_lds_dwordx4 %0, %1" :: "v"(off), "s"(base), "s"(lds) : "memory");
}
DI void lds_bar() { asm volatile("s_waitcnt lgkmcnt(0)\n\ts_barrier" ::: "memory"); }
DI void vm_wait() { asm volatile("s_waitcnt vmcnt(0)" ::: "memory"); }

DI void write_copies(LAS unsigned char* kc  , const u32* d) {
    u32 o[5];
#pragma unroll
    for (int i = 0; i < 5; ++i) o[i] = __builtin_amdgcn_alignbit(d[i + 1], d[i], 16);
    *(LAS v4u*)(kc) = (v4u){d[2], d[3], d[4], d[5]};
    *(LAS v4u*)(kc + KC_S) = (v4u){o[1], o[2], o[3], o[4]};
    *(LAS v4u*)(kc + 2 * KC_S) = (v4u){d[1], d[2], d[3], d[4]};
    *(LAS v4u*)(kc + 3 * KC_S) = (v4u){o[0], o[1], o[2], o[3]};
}

template <bool BF> DI f4 mm(v4u a, v4u b, f4 c) {
    if constexpr (BF) return __builtin_amdgcn_mfma_f32_16x16x32_bf16(__builtin_bit_cast(bf8, a), __builtin_bit_cast(bf8, b), c, 0, 0, 0);
    else return __builtin_amdgcn_mfma_f32_16x16x32_f16(__builtin_bit_cast(h8, a), __builtin_bit_cast(h8, b), c, 0, 0, 0);
}
DI v2u tr_read(LAS const unsigned char* p) { return __builtin_bit_cast(v2u, __builtin_amdgcn_ds_read_tr16_b64_v4i16((LAS s4*)p)); }

template <int P0 = 0, int P1 = 8> DI void load_afrags(v4u (&a)[2][8], LAS const unsigned char* abase) {
#pragma unroll
    for (int p = P0; p < P1; ++p)
#pragma unroll
        for (int ri = 0; ri < 2; ++ri) {
            const v2u lo = tr_read(abase + 512 * p + 64 * ri + 8), hi = tr_read(abase + 512 * p + 64 * ri);
            a[ri][p] = (v4u){lo.x, lo.y, hi.x, hi.y};
        }
}
template <int Q0> DI void load_b(v4u (&bq)[2][4], LAS const unsigned char* bbase) {
#pragma unroll
    for (int q = 0; q < 4; ++q) {
#pragma unroll
        for (int ri = 0; ri < 2; ++ri) { bq[ri][q] = *(LAS const v4u*)(bbase + 512 * (Q0 + q) + 64 * (1 - ri)); SB(); }
    }
}
template <bool BF, int Q0, int q, int TA, int TB> DI void conv_q(const v4u (&a)[2][8], const v4u (&bq)[2][4], f4 (&acc)[8]) {
#pragma unroll
    for (int ri = 0; ri < 2; ++ri)
#pragma unroll
        for (int T = TA; T < TB; ++T) acc[T] = mm<BF>(a[ri][T - Q0 - q], bq[ri][q], acc[T]);
}
DI void put_tile(f4 v, LAS unsigned char* pbase, int T) { *(LAS f4*)(pbase + (T & 3) * 1024) = v; }
template <bool BF, int Q0, int PART> DI void conv_diag(const v4u (&a)[2][8], const v4u (&bq)[2][4], f4 (&acc)[8], LAS unsigned char* pbase) {
    if constexpr (PART == 0) {
        conv_q<BF, Q0, 0, Q0, Q0 + 4>(a, bq, acc);
        SB();
        conv_q<BF, Q0, 1, Q0 + 1, Q0 + 4>(a, bq, acc);
        SB();
        put_tile(acc[Q0], pbase, Q0);
        asm volatile("" : "+v"(acc[Q0 + 1]), "+v"(acc[Q0 + 2]), "+v"(acc[Q0 + 3]));
        SB();
    } else {
        conv_q<BF, Q0, 2, Q0 + 2, Q0 + 4>(a, bq, acc);
        SB();
        put_tile(acc[Q0 + 1], pbase, Q0 + 1);
        acc[Q0 + 3] = mm<BF>(a[0][0], bq[0][3], acc[Q0 + 3]);
        SB();
        put_tile(acc[Q0 + 2], pbase, Q0 + 2);
        acc[Q0 + 3] = mm<BF>(a[1][0], bq[1][3], acc[Q0 + 3]);
        SB();
    }
}
template <bool BF, int PART> DI void conv_off(const v4u (&a)[2][8], const v4u (&bq)[2][4], f4 (&acc)[8]) {
    conv_q<BF, 0, 2 * PART, 4, 8>(a, bq, acc);
    conv_q<BF, 0, 2 * PART + 1, 4, 8>(a, bq, acc);
    asm volatile("" : "+v"(acc[4]), "+v"(acc[5]), "+v"(acc[6]), "+v"(acc[7]));
    SB();
}
DI u32 pk_bf_s(float a, float b) { u32 r; asm("v_cvt_pk_bf16_f32 %0, %1, %2" : "=v"(r) : "v"(a), "v"(b)); return r; }
DI f4 sp4(float a) { return (f4){a, a, a, a}; }

DI f2 cmul(f2 a, f2 b) {
    f2 r;
    asm("v_pk_mul_f32 %0, %1, %2 op_sel:[0,0] op_sel_hi:[0,1]\n\t"
        "v_pk_fma_f32 %0, %1, %2, %0 op_sel:[1,1,0] op_sel_hi:[1,0,1] neg_lo:[0,1,0]" : "=&v"(r) : "v"(a), "v"(b));
    return r;
}
DI f2 cexp_p(float zr2, float fh, float fl, float p) {
    const float mag = __builtin_amdgcn_exp2f(zr2 * p);
    const float an = fmaf(fl, p, __builtin_amdgcn_fractf(fh * p));
    return (f2){__builtin_amdgcn_cosf(an), __builtin_amdgcn_sinf(an)} * sp2(mag);
}
DI float exp_acc(float x) {
    const float L = 1.44269504088896340736f, Ll = 1.92596299112661746e-08f;
    const float h = x * L;
    const float lo = fmaf(x, Ll, fmaf(x, L, -h));
    const float e = __builtin_amdgcn_exp2f(h);
    return fmaf(e * lo, 0.69314718055994530942f, e);
}

template <bool V> struct BC { static constexpr bool v = V; };
#define STAMP(i) do {} while (0)

__global__ void __launch_bounds__(512, 2)
k1_main(const u16* __restrict__ qin, const u16* __restrict__ kin, const u16* __restrict__ vin, const u16* __restrict__ shk,
        const u16* __restrict__ ssmk, const u16* __restrict__ ssmD, const u16* __restrict__ lar, const u16* __restrict__ aim,
        const u16* __restrict__ cre, const u16* __restrict__ cim, const u16* __restrict__ ldt, const u16* __restrict__ dsk,
        u16* __restrict__ out, unsigned char* ws) {
    const u32 rt_start = rtime();
    SB();
    extern __shared__ __attribute__((aligned(16))) unsigned char smem[];
    LAS unsigned char* L = (LAS unsigned char*)smem;
    const int t = threadIdx.x, l = t & 63;
    const int w = __builtin_amdgcn_readfirstlane(t >> 6);
    const int wg = (blockIdx.x & 7) * 32 + (blockIdx.x >> 3);
    const int H = (wg >> 1) & 1;
    auto body = [&](auto hc, auto wc) __attribute__((always_inline)) {
    constexpr bool HH = decltype(hc)::v, WC = decltype(wc)::v;
    asm volatile("" ::: "memory");
    const int rbk = HH * 128 + (wg >> 2) * 2 + (wg & 1);
    constexpr int c1 = WC;
    const int m = t & 255;
    const int rq = w & 3;
    const int n16 = l & 15, g = l >> 4;
    const __amdgpu_buffer_rsrc_t box = __builtin_amdgcn_make_buffer_rsrc(ws, 0, (int)(WS_FLAG + 32768), 0x00020000);
    STAMP(0);
    const v4u sent4 = {SENT, SENT, SENT, SENT};
    const u32 pub_row = (u32)((t >> 2) * 16384 + rbk * 64 + (t & 3) * 16);
    {
        const u32 vo = (u32)l * 16u;
        glds16(kin + (size_t)(8 * rbk + w) * DM, vo, (u32)(L_P + (3 + w) * KV_S));
        glds16(vin + (size_t)(8 * rbk + w) * DM, vo, (u32)(L_P + (11 + w) * KV_S));
        if (!WC && w < 3) {
            if (rbk > 0) glds16(kin + (size_t)(8 * rbk - 3 + w) * DM, vo, (u32)(L_P + w * KV_S));
            else *(LAS v4u*)(L + L_P + w * KV_S + l * 16) = (v4u){0u, 0u, 0u, 0u};
        }
    }
    const int cp = t >> 1, hh = t & 1;
    const v4u sw = *(const v4u*)(shk + cp * 8);
    FENCE();
    const int pch = 2 * wg + ((w >> 1) & 1), pn = t & 127;
    const u32 r_lar = lar[pch * NS + pn], r_aim = aim[pch * NS + pn], r_cre = cre[pch * NS + pn], r_cim = cim[pch * NS + pn];
    const u32 r_cre2 = cre[pch * NS + (pn ^ 64)], r_cim2 = cim[pch * NS + (pn ^ 64)];
    u32 vz; asm volatile("v_mov_b32 %0, 0" : "=v"(vz));
    const u32 r_ldt = ldt[pch + vz];
    const u32 dA2 = *(const u32*)(ssmD + 2 * wg + vz), dB2 = *(const u32*)(dsk + 2 * wg + vz);
    FENCE();
    const int chg = 2 * wg + c1;
    const v4u fcur = *(const v4u*)(ssmk + (size_t)chg * SEQ + 8 * m);
    v2u fprv = *(const v2u*)(ssmk + (size_t)chg * SEQ + (m > 0 ? 8 * m - 4 : 0));
    FENCE();
    const u32 ini_y = (u32)((rbk >> 1) * 16384 + w * 2048 + (rbk & 1) * 1024 + l * 16);
    const u32 ini_u = (u32)((wg >> 1) * 16384 + w * 2048 + (wg & 1) * 1024 + l * 16);
    box_put(sent4, box, ini_y, 1 * MB2);
    FENCE();
    const u32 c_s = now(), r_s = rtime();
    const int row = 8 * rbk + w;
    v2u nonce;
    {
        const unsigned long long id = amd_dispatch_id(), dp = (unsigned long long)__builtin_amdgcn_dispatch_ptr();
        const u32 a = (u32)id * 0x9E3779B1u + (u32)(dp >> 32);
        const u32 b = ((u32)dp ^ ((u32)(id >> 32) * 0x85EBCA77u)) & ~1u;
        nonce = (v2u){a, b | (~a & 1u)};
    }
    const u32 flag_w = t < 8 ? WS_FLAG + (u32)(t * 256 + rbk) * 16 : 0x7FFFFFF0u;
    FENCE();
    if (m < 30) *(LAS v4u*)(L + L_U + c1 * U_S + 16 * m) = (v4u){0u, 0u, 0u, 0u};
    if (m < 8) *(LAS v4u*)(L + L_KC + (c1 * 4 + (m >> 1)) * KC_S + (m & 1) * 16) = (v4u){0u, 0u, 0u, 0u};

    LAS const unsigned char* abase = L + L_KC + (c1 * 4 + (n16 >> 2)) * KC_S + 56 - 16 * g + 8 * (n16 & 3) + 128 * rq;
    LAS const unsigned char* bbase = L + L_U + c1 * U_S + 384 + 32 * n16 + 16 * g - 128 * rq;
    LAS unsigned char* pbase = L + L_P + (rq * 2 + c1) * 4096 + (g * 16 + ((n16 + 4 * g) & 15)) * 16;
    const int eg = l & 3, en = l >> 2;
    LAS const unsigned char* prd = L + L_P + rq * 1024 + (eg * 16 + ((en + 4 * eg) & 15)) * 16;
    LAS const unsigned char* urd = L + L_U + 480 + w * 512 + l * 8;
    LAS unsigned char* ust = L + L_U + (t & 1) * U_S + 480 + (t >> 1) * 16;
    LAS unsigned char* kcw = L + L_KC + c1 * 4 * KC_S + 32 + 16 * m;
    LAS u32* ti = (LAS u32*)(L + L_TI) + (4 * (t & 1)) * 260 + (t >> 1);
    LAS const unsigned char* tr = L + L_TO + ((l & 15) >> 2) * 1088 + (w * 64 + (l >> 4) * 16 + (l & 3) * 4) * 2;
    const u32 pub_col = (u32)((w * 16 + en) * 16384 + wg * 64 + eg * 16);
    const u32 get_p = (u32)((t >> 1) * 64 + (t & 1) * 16);
    const unsigned char* gu = ws + (wg >> 1) * 16384 + (wg & 1) * 32;
    const unsigned char* gy = ws + (rbk >> 1) * 16384 + (rbk & 1) * 32;
    const unsigned char* flag_b = ws + WS_FLAG + (wg >> 5) * 4096;
    const u32 flag_o = (u32)(t >> 1) * 16;

    {
        f2 w0 = {lo2f(sw.x), lo2f(sw.z)}, w1 = {hi2f(sw.x), hi2f(sw.z)}, w2 = {lo2f(sw.y), lo2f(sw.w)}, w3 = {hi2f(sw.y), hi2f(sw.w)};
        asm volatile("" : "+v"(w0), "+v"(w1), "+v"(w2), "+v"(w3));
        lds_bar();
        STAMP(1);
        LAS const unsigned char* kp = L + L_P + 4 * hh * KV_S + cp * 4;
        u32 kr[7], vr[4];
#pragma unroll
        for (int i = 0; i < 7; ++i) kr[i] = *(LAS const u32*)(kp + i * KV_S);
#pragma unroll
        for (int i = 0; i < 4; ++i) vr[i] = *(LAS const u32*)(kp + (11 + i) * KV_S);
        f2 kf[7], kv[4];
#pragma unroll
        for (int i = 0; i < 7; ++i) kf[i] = up2(kr[i]);
#pragma unroll
        for (int r = 0; r < 4; ++r) kv[r] = fma2(kf[r + 3], w0, fma2(kf[r + 2], w1, fma2(kf[r + 1], w2, kf[r] * w3))) * up2(vr[r]);
        const u32 p00 = pk_bf(kv[0].x, kv[1].x), p01 = pk_bf(kv[2].x, kv[3].x);
        const u32 p10 = pk_bf(kv[0].y, kv[1].y), p11 = pk_bf(kv[2].y, kv[3].y);
        const u32 s0 = hh ? p00 : p10, s1 = hh ? p01 : p11;
        const u32 r0 = dppu<0xB1>(s0), r1 = dppu<0xB1>(s1);
        v4u o = hh ? (v4u){r0, r1, p10, p11} : (v4u){p00, p01, r0, r1};
        asm volatile("" : "+v"(o));
            box_put(o, box, pub_row, 0);
        FENCE();
    }
    STAMP(2); STAMP(3);
    f2 C, e128, ea, eb;
    {
        const float dt = rb(exp_acc(bf2f(r_ldt)));
        const float are = rb(exp_acc(bf2f(r_lar)));
        const float zr2 = -(are * dt) * 1.44269504088896340736f;
        const float zi = bf2f(r_aim) * dt;
        const float chi = 0.15915494309189533577f, clo = 6.420638326565253e-09f;
        const float fh = zi * chi;
        const float fl2 = fmaf(zi, clo, fmaf(zi, chi, -fh));
        const u32 am = max(max(r_cre & 0x7FFFu, r_cim & 0x7FFFu), max(r_cre2 & 0x7FFFu, r_cim2 & 0x7FFFu));
        const float mx = bf2f(wave_maxu(am)) * dt;
        int ec = __builtin_amdgcn_frexp_expf(mx);
        ec = ec < -100 ? -100 : ec;
        const float sA = __builtin_amdgcn_ldexpf(1.0f, 13 - ec);
        C = (f2){bf2f(r_cre), -bf2f(r_cim)} * sp2(dt) * sp2(sA);
        e128 = cexp_p(zr2, -fh, -fl2, 128.f);
        ea = cexp_p(zr2, -fh, -fl2, WC ? 1.f : 4.f);
        eb = cexp_p(zr2, -fh, -fl2, WC ? 2.f : 512.f);
        if (l == 0) *(LAS int*)(L + L_KCMP + ((w >> 1) & 1) * KCMP_S + 2824) = ec;
        asm volatile("" : "+v"(C), "+v"(e128), "+v"(ea), "+v"(eb));
        SB();
    }
    STAMP(4);
    {
        if (m == 0) fprv = (v2u){0u, 0u};
        const u32 d[6] = {fprv.x, fprv.y, fcur.x, fcur.y, fcur.z, fcur.w};
        write_copies(kcw, d);
    }
    FENCE();
    v4u fl, q16;
    v4u af[2][8];
    v4u pc;
    {
        LAS unsigned char* tb = L + L_TB + ((w >> 1) & 1) * TB_S + pn * 4;
#define PUT(rowi, v) *(LAS u32*)(tb + (rowi) * TB_R) = pk_h((v).x, (v).y)
#define MIDDLE() do { \
        SB(); \
        vm_wait();                 \
        lds_bar(); \
        if (!WC && w == 0) box_put((v4u){nonce.x, nonce.y, rtime(), 0u}, box, flag_w, 0); \
        tfl = now(); \
        q16 = *(const v4u*)(qin + (size_t)row * DM + 8 * l); \
        FENCE(); \
        STAMP(5); \
        load_afrags(af, abase); } while (0)
#define MIDDLE2() do { \
        SB(); \
        wait_until(tfl, D_FL + (31 - (blockIdx.x >> 3)) * FL_K); \
        fl = box_req(flag_b, flag_o); \
        FENCE(); } while (0)
        u32 tfl;
        if constexpr (!WC) {
            f2 G = cmul(C, eb);
            f2 e8 = cmul(ea, ea); e8.y = -e8.y;
            f2 B = {16384.f, 0.f};
#pragma unroll
            for (int r = 0; r < 16; ++r) {
                const int h = r + 4;
                if (r == MID_A) { asm volatile("" : "+v"(G), "+v"(B), "+v"(e8)); MIDDLE(); }
                if (r == MID_B) { asm volatile("" : "+v"(G), "+v"(B), "+v"(e8)); MIDDLE2(); }
                PUT(41 + r, B); if (r < 15) B = cmul(B, e8);
                if (h < 8) { PUT(24 + 2 * (h - 4), G); const f2 G4 = cmul(G, ea); PUT(25 + 2 * (h - 4), G4); }
                else if (h <= 16) PUT(24 + h, G);
                if (h < 16) G = cmul(G, e128);
            }
        } else {
            f2 e[8];
            e[1] = ea; e[2] = eb; e[3] = cmul(e[2], e[1]); e[4] = cmul(e[2], e[2]);
            e[5] = cmul(e[4], e[1]); e[6] = cmul(e[4], e[2]); e[7] = cmul(e[4], e[3]);
            f2 G = C;
#pragma unroll
            for (int h = 0; h < 4; ++h) {
                if (h == MID_C) { asm volatile("" : "+v"(G)); MIDDLE(); }
                if (h == MID_D) { asm volatile("" : "+v"(G)); MIDDLE2(); }
                if (h < 2) {
                    PUT(8 * h, G);
#pragma unroll
                    for (int a = 1; a < 8; ++a) { const f2 v = cmul(G, e[a]); PUT(8 * h + a, v); }
                } else {
                    PUT(16 + 4 * (h - 2), G);
#pragma unroll
                    for (int a = 1; a < 4; ++a) { const f2 v = cmul(G, e[2 * a]); PUT(16 + 4 * (h - 2) + a, v); }
                }
                if (h < 3) G = cmul(G, e128);
            }
        }
#undef PUT
    }
    STAMP(6);

    f4 acc[8];
    v4u bq[2][4];
    v4u py, py2;
#define EPILOGUE(BOXI, S0, S1) do { \
        f4 pp[8]; \
        _Pragma("unroll") for (int i = 0; i < 8; ++i) pp[i] = *(LAS const f4*)(prd + i * 4096); \
        const f4 y0 = __builtin_elementwise_fma((pp[0] + pp[2]) + (pp[4] + pp[6]), sp4(S0), e0); \
        const f4 y1 = __builtin_elementwise_fma((pp[1] + pp[3]) + (pp[5] + pp[7]), sp4(S1), e1); \
        v4u o; \
        _Pragma("unroll") for (int r = 0; r < 4; ++r) o[r] = clampS(pk_bf_s(y0[r], y1[r])); \
        box_put(o, box, pub_col, (BOXI) * MB2); \
        FENCE(); \
        __builtin_amdgcn_s_setprio(0); } while (0)
#define ZERO_ACC() do { _Pragma("unroll") for (int i = 0; i < 8; ++i) acc[i] = (f4){0.f, 0.f, 0.f, 0.f}; } while (0)
#define PUT_TI(p, o) do { ti[(o)] = p.x; ti[260 + (o)] = p.y; ti[520 + (o)] = p.z; ti[780 + (o)] = p.w; } while (0)
#define KGEN_MM(ch, mt, bfr) do { \
        LAS const unsigned char* ta = L + L_TB + (ch) * TB_S + (16 * (mt) + n16) * TB_R + 16 * g; \
        v4u afr[8]; \
        _Pragma("unroll") for (int s = 0; s < 8; ++s) afr[s] = *(LAS const v4u*)(ta + 64 * s); \
        f4 d0 = {0.f, 0.f, 0.f, 0.f}, d1 = {0.f, 0.f, 0.f, 0.f}; \
        _Pragma("unroll") for (int s = 0; s < 8; s += 2) { d0 = mm<false>(afr[s], bfr[s], d0); d1 = mm<false>(afr[s + 1], bfr[s + 1], d1); } \
        const f4 dacc = d0 + d1; \
        const int pb = 8 * n16 + ((mt) == 0 ? 4 * (g & 1) + 128 * (g >> 1) : (mt) == 1 ? (g < 2 ? 256 + 128 * g : 256 * g) : 1024 + 512 * g); \
        const int s1 = (mt) == 0 ? 1 : (mt) == 1 ? (g < 2 ? 2 : 4) : 128, s2 = (mt) == 0 ? 2 : (mt) == 1 ? (g < 2 ? 4 : 128) : 256; \
        _Pragma("unroll") for (int i = 0; i < 4; ++i) { \
            int p = pb + (i & 1) * s1 + (i >> 1) * s2; \
            p = p > 2048 ? 2064 : p; \
            u32 ad = __builtin_bit_cast(u32, (float)p) >> 15; \
            if (i == 0) ad = ad > 0x7EFEu ? ad : 0x7EFEu; \
            *(LAS _Float16*)(L + L_KCMP + (ch) * KCMP_S - 0x7EFE + ad) = (_Float16)(dacc[i] * 1.9073486328125e-06f);     \
        } } while (0)
#define EXPAND() do { \
        u32 d[6]; \
        const float f0 = (float)(8 * m - 4); \
        const u32 kb = (u32)(L_KCMP + c1 * KCMP_S - 2 * 0x3F7F); \
        _Pragma("unroll") for (int i = 0; i < 6; ++i) { \
            float fa = f0 + (float)(2 * i), fb = f0 + (float)(2 * i + 1); \
            if (i < 3) fa = fmaxf(fa, 0.99609375f); \
            if (i < 2) fb = fmaxf(fb, 0.99609375f); \
            const u32 cd = pk_bf(fa, fb); \
            u32 a0, a1; \
            asm("v_mad_u32_u16 %0, %2, 2, %3\n\tv_mad_u32_u16 %1, %2, 2, %3 op_sel:[1,0,0,0]" : "=&v"(a0), "=v"(a1) : "v"(cd), "s"(kb)); \
            us2 v; v.x = *(LAS const u16*)(L + a0); v.y = *(LAS const u16*)(L + a1); \
            d[i] = __builtin_bit_cast(u32, v); \
        } \
        if (m == 0) { d[0] = 0u; d[1] = 0u; } \
        write_copies(kcw, d); \
        SB(); } while (0)
    {
        asm volatile("s_waitcnt vmcnt(0)" : "+v"(fl) :: "memory");
#pragma unroll 1
        for (u32 sp = 0; sp < (1u << 16); ++sp) {
            const unsigned long long bad = __builtin_amdgcn_ballot_w64(fl.x != nonce.x || fl.y != nonce.y);
            if (!bad) break;
            unsigned long long sv;
            asm volatile("s_sleep 1\n\ts_mov_b64 %1, exec\n\ts_mov_b64 exec, %4\n\tglobal_load_dwordx4 %0, %2, %3 sc1\n\ts_mov_b64 exec, %1\n\ts_waitcnt vmcnt(0)"
                         : "+v"(fl), "=&s"(sv) : "v"(flag_o), "s"(flag_b), "s"(bad) : "memory");
        }
        asm volatile("" : "+v"(q16));
        pc = box_req(gu, get_p);
        FENCE();
        STAMP(7);
    }
    lds_bar();
    if (w < 6) {
        const int ch = w >= 3;
        LAS const unsigned char* tbb = L + L_TB + ch * TB_S + (41 + n16) * TB_R + 16 * g;
        v4u bfr[8];
#pragma unroll
        for (int s = 0; s < 8; ++s) bfr[s] = *(LAS const v4u*)(tbb + 64 * s);
        const int mt = w - 3 * ch;
        KGEN_MM(ch, mt, bfr);
    }
    SB();
    {
        const u32 c_n = now(), r_n = rtime();
        const float cpt = (float)(c_n - c_s) * __builtin_amdgcn_rcpf((float)(r_n - r_s));
        const u32 epw = c_n + (u32)(int)((float)wave_maxi((int)(fl.z - r_n)) * cpt);
        if (l == 0) *(LAS u32*)(L + L_EP + 4 * w) = epw;
    }
    SB();
    float rs0, nb0;
    u32 ep;
    {
        STAMP(8);
        asm volatile("s_waitcnt vmcnt(0)" : "+v"(pc) :: "memory");
        *(LAS v4u*)ust = pc;
        box_put(sent4, box, ini_u, 2 * MB2);
        box_put(sent4, box, ini_y, 3 * MB2);
        lds_bar();
        STAMP(9);
        load_b<0>(bq, bbase);
        const v2u i0 = *(LAS const v2u*)(urd), i1 = *(LAS const v2u*)(urd + U_S);
        ZERO_ACC();
        SB();
        conv_diag<true, 0, 0>(af, bq, acc, pbase);
        conv_diag<true, 0, 1>(af, bq, acc, pbase);
        f4 e0 = (f4){lo2f(i0.x), hi2f(i0.x), lo2f(i0.y), hi2f(i0.y)} * sp4(lo2f(dA2));
        f4 e1 = (f4){lo2f(i1.x), hi2f(i1.x), lo2f(i1.y), hi2f(i1.y)} * sp4(hi2f(dA2));
        asm volatile("" : "+v"(e0), "+v"(e1));
        SB();
        put_tile(acc[3], pbase, 3);
        if constexpr (!WC) {
            __builtin_amdgcn_s_setprio(3);
            asm volatile("s_waitcnt vmcnt(1)" ::: "memory");
            lds_bar();
            EPILOGUE(1, 1.0f, 1.0f);
        } else {
            lds_bar();
        }
        STAMP(10);
        {
            const v4u ea_ = *(LAS const v4u*)(L + L_EP), eb_ = *(LAS const v4u*)(L + L_EP + 16);
            const u32 r = ea_.x;
            const int d = max(max(max((int)(ea_.y - r), (int)(ea_.z - r)), max((int)(ea_.w - r), (int)(eb_.x - r))),
                              max(max((int)(eb_.y - r), (int)(eb_.z - r)), max((int)(eb_.w - r), 0)));
            ep = __builtin_amdgcn_readfirstlane(r + (u32)d);
        }
        v4u b2[2][4];
        conv_off<true, 0>(af, bq, acc);
        load_b<4>(b2, bbase);
        conv_off<true, 1>(af, bq, acc);
        lds_bar();
        STAMP(11);
        conv_diag<true, 4, 0>(af, b2, acc, pbase);
        if constexpr (!HH && !WC) { wait_until(ep, S_YA0); py = box_req(gy + 1 * MB2, get_p); py2 = box_req(gy + 1 * MB2 + 8192, get_p); SB(); }
        conv_diag<true, 4, 1>(af, b2, acc, pbase);
        put_tile(acc[7], pbase, 7);
        if constexpr (WC) { __builtin_amdgcn_s_setprio(3); asm volatile("s_waitcnt vmcnt(1)" ::: "memory"); }
        lds_bar();
        if constexpr (WC) EPILOGUE(1, 1.0f, 1.0f);
        STAMP(12);
        box_put(sent4, box, ini_u, 4 * MB2);
        FENCE();
        const f2 xq[4] = {up2(q16.x), up2(q16.y), up2(q16.z), up2(q16.w)};
        ln_stats(xq, 1.0f, rs0, nb0);
    }
    const float sc0 = __builtin_amdgcn_ldexpf(1.0f / USCALE, *(LAS const int*)(L + L_KCMP + 2824) - 7);
    const float sc1 = __builtin_amdgcn_ldexpf(1.0f / USCALE, *(LAS const int*)(L + L_KCMP + KCMP_S + 2824) - 7);

    v4u yAp;
    f2 x[4] = {up2(q16.x), up2(q16.y), up2(q16.z), up2(q16.w)};
#pragma unroll
    for (int it = 0; it < 3; ++it) {
        {
            f2 gate[4];
            if (it == 0) {
#pragma unroll
                for (int i = 0; i < 4; ++i) gate[i] = fma2(x[i], sp2(rs0), sp2(nb0));
            } else if (it == 1) { gate[0] = up2(yAp.x); gate[1] = up2(yAp.y); gate[2] = up2(yAp.z); gate[3] = up2(yAp.w); }
            if (it < 2) asm volatile("" : "+v"(gate[0]), "+v"(gate[1]), "+v"(gate[2]), "+v"(gate[3]));
            if constexpr (HH) {
                if (it == 0) { EXPAND(); lds_bar(); }
                wait_until(ep, it == 0 ? S_YA1 : it == 1 ? S_Y10 : S_Y11);
                py = box_req(gy + (1 + 2 * it) * MB2, get_p);
                if (it == 0) { SB(); load_afrags(af, abase); SB(); }
                py = box_fin(0, py, gy + (1 + 2 * it) * MB2, get_p, 1 + 4 * it);
                PUT_TI(py, 0);
                if (it < 2 && !WC) { wait_until(ep, it ? S_U01 : S_U00); pc = box_req(gu + (2 + 2 * it) * MB2, get_p); }
            } else if constexpr (!WC) {
                py = box_fin(it == 0 ? 2 : 1, py, gy + (1 + 2 * it) * MB2, get_p, 4 * it);
                PUT_TI(py, 0);
                py2 = box_fin(it == 0 ? 1 : 0, py2, gy + (1 + 2 * it) * MB2 + 8192, get_p, 4 * it);
                PUT_TI(py2, 128);
            } else {
                asm volatile("s_waitcnt vmcnt(%0)" :: "i"(it == 0 ? 2 : 1) : "memory");
            }
            lds_bar();
            STAMP(13 + 7 * it);
            const v4u yv = *(LAS const v4u*)(L + L_TI + w * 1040 + l * 16);
            const f2 yc[4] = {up2(yv.x), up2(yv.y), up2(yv.z), up2(yv.w)};
            float rs, nb;
            if (it == 0) {
#pragma unroll
                for (int i = 0; i < 4; ++i) x[i] = fma2(gate[i], yc[i], x[i]);
                yAp = yv;
            } else {
#pragma unroll
                for (int i = 0; i < 4; ++i) x[i] += yc[i];
            }
            if (it == 2) {
#pragma unroll 1
                for (int i = 0; i < 4096; ++i) if ((int)(rtime() - rt_start - 2500) >= 0) break;
                *(v4u*)(out + (size_t)row * DM + 8 * l) = (v4u){pk_bf(x[0].x, x[0].y), pk_bf(x[1].x, x[1].y), pk_bf(x[2].x, x[2].y), pk_bf(x[3].x, x[3].y)};
                STAMP(14 + 7 * it);
                break;
            }
            if (it == 1) {
                ln_stats(x, 1.0f, rs, nb);
#pragma unroll
                for (int i = 0; i < 4; ++i) x[i] = fma2(fma2(x[i], sp2(rs), sp2(nb)), gate[i], x[i]);
            }
            ln_stats(x, USCALE, rs, nb);
            v4u uo;
#pragma unroll
            for (int i = 0; i < 4; ++i) { const f2 u = fma2(x[i], sp2(rs), sp2(nb)); uo[i] = pk_h(u.x, u.y); }
            *(LAS v4u*)(L + L_TO + w * 1088 + l * 16) = uo;
            lds_bar();
            const v2u lo2 = tr_read(tr), hi2 = tr_read(tr + 4 * 1088);
            box_put((v4u){clampS(lo2.x), clampS(lo2.y), clampS(hi2.x), clampS(hi2.y)}, box, pub_row, (2 + 2 * it) * MB2);
            FENCE();
            STAMP(14 + 7 * it);
        }
        {
            if constexpr (HH) {
                if constexpr (!WC) { pc = box_fin(1, pc, gu + (2 + 2 * it) * MB2, get_p, 2 + 4 * it); *(LAS v4u*)ust = pc; }
            } else {
                if (it == 0) { EXPAND(); lds_bar(); }
                if constexpr (!WC) { wait_until(ep, it ? S_U01 : S_U00); pc = box_req(gu + (2 + 2 * it) * MB2, get_p); }
                if (it == 0) { SB(); load_afrags(af, abase); SB(); }
                if constexpr (!WC) { pc = box_fin(0, pc, gu + (2 + 2 * it) * MB2, get_p, 2 + 4 * it); *(LAS v4u*)ust = pc; }
            }
            lds_bar();
            STAMP(15 + 7 * it);
            load_b<0>(bq, bbase);
            const float d0 = lo2f(dB2) * (1.0f / USCALE), d1 = hi2f(dB2) * (1.0f / USCALE);
#define SKIP_LOAD() const v2u i0 = *(LAS const v2u*)(urd), i1 = *(LAS const v2u*)(urd + U_S)
#define SKIP_TERMS() \
            const h2 h00 = as_h2(i0.x), h01 = as_h2(i0.y), h10 = as_h2(i1.x), h11 = as_h2(i1.y); \
            f4 e0 = (f4){(float)h00.x, (float)h00.y, (float)h01.x, (float)h01.y} * sp4(d0); \
            f4 e1 = (f4){(float)h10.x, (float)h10.y, (float)h11.x, (float)h11.y} * sp4(d1); \
            asm volatile("" : "+v"(e0), "+v"(e1)); SB()
            ZERO_ACC();
            if constexpr (!WC) {
                SKIP_LOAD();
                SB();
                conv_diag<false, 0, 0>(af, bq, acc, pbase);
                conv_diag<false, 0, 1>(af, bq, acc, pbase);
                SKIP_TERMS();
                put_tile(acc[3], pbase, 3);
                __builtin_amdgcn_s_setprio(3);
                lds_bar();
                STAMP(16 + 7 * it);
                EPILOGUE(3 + 2 * it, sc0, sc1);
            } else {
                SB();
                conv_diag<false, 0, 0>(af, bq, acc, pbase);
                conv_diag<false, 0, 1>(af, bq, acc, pbase);
                put_tile(acc[3], pbase, 3);
                if constexpr (HH) { wait_until(ep, it ? S_U11 : S_U10); pc = box_req(gu + (2 + 2 * it) * MB2, get_p); }
                lds_bar();
                STAMP(16 + 7 * it);
            }
            if (it == 0 && !(HH && WC)) {
                box_put(sent4, box, ini_y, 5 * MB2);
                if (!WC && w == 0) __builtin_amdgcn_raw_buffer_store_b64(~nonce, box, flag_w, 0, 16);
                FENCE();
            }
            conv_off<false, 0>(af, bq, acc);
            if constexpr (WC && !HH) { wait_until(ep, it ? S_U11 : S_U10); pc = box_req(gu + (2 + 2 * it) * MB2, get_p); SB(); }
            conv_off<false, 1>(af, bq, acc);
            if constexpr (WC) {
                pc = box_fin(0, pc, gu + (2 + 2 * it) * MB2, get_p, 3 + 4 * it);
                *(LAS v4u*)ust = pc;
                if (it == 0 && HH) {
                    box_put(sent4, box, ini_y, 5 * MB2);
                    FENCE();
                }
            }
            STAMP(17 + 7 * it);
            lds_bar();
            STAMP(18 + 7 * it);
            load_b<4>(bq, bbase);
            if constexpr (WC) {
                SKIP_LOAD();
                SB();
                conv_diag<false, 4, 0>(af, bq, acc, pbase);
                conv_diag<false, 4, 1>(af, bq, acc, pbase);
                SKIP_TERMS();
                put_tile(acc[7], pbase, 7);
                __builtin_amdgcn_s_setprio(3);
                lds_bar();
                EPILOGUE(3 + 2 * it, sc0, sc1);
            } else {
                conv_diag<false, 4, 0>(af, bq, acc, pbase);
                if constexpr (!HH) {
                    wait_until(ep, it ? S_Y01 : S_Y00);
                    py = box_req(gy + (3 + 2 * it) * MB2, get_p); py2 = box_req(gy + (3 + 2 * it) * MB2 + 8192, get_p); SB();
                }
                conv_diag<false, 4, 1>(af, bq, acc, pbase);
                put_tile(acc[7], pbase, 7);
                lds_bar();
            }
            STAMP(19 + 7 * it);
        }
    }
    };
    if (H) { if (w < 4) body(BC<true>{}, BC<false>{}); else body(BC<true>{}, BC<true>{}); }
    else   { if (w < 4) body(BC<false>{}, BC<false>{}); else body(BC<false>{}, BC<true>{}); }
}

extern "C" void kernel_launch(void* const* d_in, const int* in_sizes, int n_in, void* d_out, int out_size, void* d_ws, size_t ws_size,
                              hipStream_t stream) {
    (void)hipFuncSetAttribute((const void*)k1_main, hipFuncAttributeMaxDynamicSharedMemorySize, L_END);
    k1_main<<<256, 512, L_END, stream>>>((const u16*)d_in[0], (const u16*)d_in[1], (const u16*)d_in[2], (const u16*)d_in[3], (const u16*)d_in[4],
                                         (const u16*)d_in[5], (const u16*)d_in[7], (const u16*)d_in[8], (const u16*)d_in[9], (const u16*)d_in[10],
                                         (const u16*)d_in[11], (const u16*)d_in[12], (u16*)d_out, (unsigned char*)d_ws);
}
```

```cpp
#include <hip/hip_runtime.h>

typedef unsigned short u16;
typedef unsigned u32;
typedef u32 v4u __attribute__((ext_vector_type(4)));
typedef u32 v2u __attribute__((ext_vector_type(2)));
typedef float f4 __attribute__((ext_vector_type(4)));
typedef float f2 __attribute__((ext_vector_type(2)));
typedef __bf16 bf8 __attribute__((ext_vector_type(8)));
typedef __bf16 bf2 __attribute__((ext_vector_type(2)));
typedef _Float16 h8 __attribute__((ext_vector_type(8)));
typedef _Float16 h2 __attribute__((ext_vector_type(2)));
typedef short s4 __attribute__((ext_vector_type(4)));
typedef unsigned short us2 __attribute__((ext_vector_type(2)));
#define LAS __attribute__((address_space(3)))
#define DI __device__ __forceinline__
extern "C" __device__ unsigned long long amd_dispatch_id() __asm("llvm.amdgcn.dispatch.id");

constexpr int SEQ = 2048, DM = 512, NS = 128;
constexpr u32 SENT = 0xFFFFFFFFu;
constexpr u32 MB2 = 2u * 1024u * 1024u;
constexpr u32 WS_FLAG = 6 * MB2;
constexpr size_t WS_STAMP = 8 * (size_t)MB2;
constexpr float USCALE = 16.0f;
constexpr u32 WS_HINT = WS_FLAG + 32768;
#define D_FL 700
#define MID_A 0
#define MID_B 8
#define MID_C 0
#define MID_D 2
#define FL_K 25
#define T_VIS 38
#define T_RD 48
#define T_H1 35
#define C_E0A 1500
#define C_E0 1320
#define C_A1 3200
#define C_01 2600
#define C_E1 1450
#define C_R0 1050
#define C_R1 1400
#define C_UA 2900
#define C_UB 3200
constexpr u32 HW = 800;

constexpr int KC_S = 4160;
constexpr int L_KC = 0;
constexpr int U_S = 4672;
constexpr int L_U = L_KC + 2 * 4 * KC_S;
constexpr int L_TI = L_U + 2 * U_S;
constexpr int L_TO = L_TI + 8 * 1040;
constexpr int KCMP_S = 2832;
constexpr int L_KCMP = L_TO + 8 * 1088;
constexpr int TB_R = 544, TB_S = 57 * TB_R;
constexpr int L_TB = L_KCMP + 2 * KCMP_S;
constexpr int L_P = L_TB + 2 * TB_S;
constexpr int KV_S = 1056;
constexpr int L_END = L_P + 32768;

DI float bf2f(u32 h) { return __builtin_bit_cast(float, h << 16); }
DI float lo2f(u32 d) { return __builtin_bit_cast(float, d << 16); }
DI float hi2f(u32 d) { return __builtin_bit_cast(float, d & 0xFFFF0000u); }
DI f2 up2(u32 d) { return (f2){lo2f(d), hi2f(d)}; }
DI float rb(float x) { f2 t = {0.f, x}; return __builtin_bit_cast(float, __builtin_convertvector(t, bf2)); }
DI u32 pk_bf(float a, float b) { f2 t = {a, b}; return __builtin_bit_cast(u32, __builtin_convertvector(t, bf2)); }
DI u32 pk_h(float a, float b) { f2 t = {a, b}; return __builtin_bit_cast(u32, __builtin_convertvector(t, h2)); }
DI h2 as_h2(u32 d) { return __builtin_bit_cast(h2, d); }
DI u32 clampS(u32 d) { return d < 0xFFFFFFFEu ? d : 0xFFFFFFFEu; }
DI f2 fma2(f2 a, f2 b, f2 c) { return __builtin_elementwise_fma(a, b, c); }
DI f2 sp2(float a) { return (f2){a, a}; }
template <int CTRL, int RM = 0xf> DI float dppf(float v) {
    return __builtin_bit_cast(float, __builtin_amdgcn_update_dpp(0, __builtin_bit_cast(int, v), CTRL, RM, 0xf, true));
}
DI float rdl(float v, int l) { return __builtin_bit_cast(float, __builtin_amdgcn_readlane(__builtin_bit_cast(int, v), l)); }
DI void wave_sum2(float& a, float& b) {
    const auto sw = __builtin_amdgcn_permlane32_swap(__builtin_bit_cast(u32, a), __builtin_bit_cast(u32, b), false, false);
    float r = __builtin_bit_cast(float, (u32)sw[0]) + __builtin_bit_cast(float, (u32)sw[1]);
    r += dppf<0xB1>(r); r += dppf<0x4E>(r); r += dppf<0x141>(r); r += dppf<0x140>(r);
    asm volatile("s_nop 1\n\tv_add_f32_dpp %0, %0, %0 row_bcast:15 row_mask:0xa bank_mask:0xf\n\ts_nop 0" : "+v"(r));
    a = rdl(r, 31); b = rdl(r, 63);
}
template <int CTRL, int RM = 0xf> DI u32 dppu(u32 v) { return (u32)__builtin_amdgcn_update_dpp(0, (int)v, CTRL, RM, 0xf, true); }
DI u32 wave_maxu(u32 v) {
    v = max(v, dppu<0xB1>(v)); v = max(v, dppu<0x4E>(v)); v = max(v, dppu<0x141>(v)); v = max(v, dppu<0x140>(v));
    return max(max((u32)__builtin_amdgcn_readlane(v, 0), (u32)__builtin_amdgcn_readlane(v, 16)),
               max((u32)__builtin_amdgcn_readlane(v, 32), (u32)__builtin_amdgcn_readlane(v, 48)));
}
DI void ln_stats(const f2* x, float scale, float& rs, float& nb) {
    const float c = rdl(x[0].x, 0);
    f2 d[4];
#pragma unroll
    for (int i = 0; i < 4; ++i) d[i] = x[i] - sp2(c);
    const f2 s2 = (d[0] + d[1]) + (d[2] + d[3]);
    const f2 q2 = fma2(d[3], d[3], fma2(d[2], d[2], fma2(d[1], d[1], d[0] * d[0])));
    float s = s2.x + s2.y, q = q2.x + q2.y;
    wave_sum2(s, q);
    const float md = s * (1.0f / DM);
    const float var = fmaxf(fmaf(-md, md, q * (1.0f / DM)), 0.f);
    const float mb = rb(c + md);
    const float sd = rb(__builtin_amdgcn_sqrtf(rb(rb(var) + 1e-5f)));
    rs = __builtin_amdgcn_rcpf(sd) * scale;
    nb = -mb * rs;
}

DI bool has_sent(v4u v) { const u32 a = v.x > v.y ? v.x : v.y, b = v.z > v.w ? v.z : v.w; return (a > b ? a : b) == SENT; }
#define SB() __builtin_amdgcn_sched_barrier(0)
#define FENCE() do { asm volatile("" ::: "memory"); SB(); } while (0)
DI u32 now() { unsigned long long t_; asm volatile("s_memtime %0\n\ts_waitcnt lgkmcnt(0)" : "=s"(t_) :: "memory"); return (u32)t_; }
DI void wait_until(u32 t0, u32 d) {
#pragma unroll 1
    for (int i = 0; i < 512; ++i) if ((int)(now() - t0 - d) >= 0) break;
}
DI u32 rtime() { unsigned long long t_; asm volatile("s_memrealtime %0\n\ts_waitcnt lgkmcnt(0)" : "=s"(t_) :: "memory"); return (u32)t_; }
DI void wait_rt(u32 T) {
#pragma unroll 1
    for (int i = 0; i < 1024; ++i) if ((int)(rtime() - T) >= 0) break;
}
DI v4u box_req(const unsigned char* base, u32 off) {
    v4u v = {SENT, SENT, SENT, SENT};
    asm volatile("global_load_dwordx4 %0, %1, %2 sc1" : "+v"(v) : "v"(off), "s"(base) : "memory"); return v;
}
DI void box_req_if(v4u& v, const unsigned char* base, u32 off, u32 c) {
    asm volatile("s_cmp_eq_u32 %3, 0\n\ts_cbranch_scc1 .Lrq%=\n\tglobal_load_dwordx4 %0, %1, %2 sc1\n.Lrq%=:" : "+v"(v) : "v"(off), "s"(base), "s"(__builtin_amdgcn_readfirstlane(c)) : "memory", "scc");
}
DI v4u hint_req(const unsigned char* base, u32 off) {
    v4u v; asm volatile("global_load_dwordx4 %0, %1, %2 sc1" : "=v"(v) : "v"(off), "s"(base) : "memory"); return v;
}
DI u32 hint_chk(int N, v4u h, u32& n) {
    asm volatile("s_waitcnt vmcnt(%1)" : "+v"(h) : "i"(N) : "memory");
    n = rtime();
    const u32 b = n - HW;
    const u32 mx = wave_maxu(max(max(h.x - b, h.y - b), max(h.z - b, h.w - b)));
    return mx <= 2 * HW ? b + mx : n + 0x40000000u;
}
#define RM(x)
#define RMP
#define RMS() do {} while (0)
DI void hint_wait(u32 T, u32 n, const unsigned char* base, u32 off RMP) {
#pragma unroll 1
    for (int i = 0; i < 6 && (int)(T - n) > (int)(2 * HW); ++i) {
        RMS();
        T = hint_chk(0, hint_req(base, off), n);
    }
    if ((int)(T - n) <= (int)(2 * HW)) wait_rt(T);
}
#define box_fin(N, v, base, off, id) box_fin_(N, v, base, off RM(1u << (id)))
DI v4u box_fin_(int N, v4u v, const unsigned char* base, u32 off RMP) {
    asm volatile("s_waitcnt vmcnt(%1)" : "+v"(v) : "i"(N) : "memory");
#pragma unroll 1
    for (u32 sp = 0; sp < (1u << 16); ++sp) {
        const unsigned long long bad = __builtin_amdgcn_ballot_w64(has_sent(v));
        if (!bad) break;
        RMS();
        unsigned long long sv;
        asm volatile("s_sleep 2\n\ts_mov_b64 %1, exec\n\ts_mov_b64 exec, %4\n\tglobal_load_dwordx4 %0, %2, %3 sc1\n\ts_mov_b64 exec, %1\n\ts_waitcnt vmcnt(0)"
                     : "+v"(v), "=&s"(sv) : "v"(off), "s"(base), "s"(bad) : "memory");
    }
    return v;
}
DI void box_put(v4u v, __amdgpu_buffer_rsrc_t r, u32 off, u32 soff) {
    asm volatile("" : "+v"(v));
    __builtin_amdgcn_raw_buffer_store_b128(v, r, off, soff, 16);
    asm volatile("s_nop 1" :: "v"(v) : "memory");
}
DI void glds16(const void* base, u32 off, u32 lds) {
    asm volatile("s_mov_b32 m0, %2\n\ts_nop 0\n\tglobal_load_lds_dwordx4 %0, %1" :: "v"(off), "s"(base), "s"(lds) : "memory");
}
DI void lds_bar() { asm volatile("s_waitcnt lgkmcnt(0)\n\ts_barrier" ::: "memory"); }
DI void vm_wait() { asm volatile("s_waitcnt vmcnt(0)" ::: "memory"); }

DI void write_copies(LAS unsigned char* kc  , const u32* d) {
    u32 o[5];
#pragma unroll
    for (int i = 0; i < 5; ++i) o[i] = __builtin_amdgcn_alignbit(d[i + 1], d[i], 16);
    *(LAS v4u*)(kc) = (v4u){d[2], d[3], d[4], d[5]};
    *(LAS v4u*)(kc + KC_S) = (v4u){o[1], o[2], o[3], o[4]};
    *(LAS v4u*)(kc + 2 * KC_S) = (v4u){d[1], d[2], d[3], d[4]};
    *(LAS v4u*)(kc + 3 * KC_S) = (v4u){o[0], o[1], o[2], o[3]};
}

template <bool BF> DI f4 mm(v4u a, v4u b, f4 c) {
    if constexpr (BF) return __builtin_amdgcn_mfma_f32_16x16x32_bf16(__builtin_bit_cast(bf8, a), __builtin_bit_cast(bf8, b), c, 0, 0, 0);
    else return __builtin_amdgcn_mfma_f32_16x16x32_f16(__builtin_bit_cast(h8, a), __builtin_bit_cast(h8, b), c, 0, 0, 0);
}
DI v2u tr_read(LAS const unsigned char* p) { return __builtin_bit_cast(v2u, __builtin_amdgcn_ds_read_tr16_b64_v4i16((LAS s4*)p)); }

template <int P0 = 0, int P1 = 8> DI void load_afrags(v4u (&a)[2][8], LAS const unsigned char* abase) {
#pragma unroll
    for (int p = P0; p < P1; ++p)
#pragma unroll
        for (int ri = 0; ri < 2; ++ri) {
            const v2u lo = tr_read(abase + 512 * p + 64 * ri + 8), hi = tr_read(abase + 512 * p + 64 * ri);
            a[ri][p] = (v4u){lo.x, lo.y, hi.x, hi.y};
        }
}
template <int Q0> DI void load_b(v4u (&bq)[2][4], LAS const unsigned char* bbase) {
#pragma unroll
    for (int q = 0; q < 4; ++q) {
#pragma unroll
        for (int ri = 0; ri < 2; ++ri) { bq[ri][q] = *(LAS const v4u*)(bbase + 512 * (Q0 + q) + 64 * (1 - ri)); SB(); }
    }
}
template <bool BF, int Q0, int q, int TA, int TB> DI void conv_q(const v4u (&a)[2][8], const v4u (&bq)[2][4], f4 (&acc)[8]) {
#pragma unroll
    for (int ri = 0; ri < 2; ++ri)
#pragma unroll
        for (int T = TA; T < TB; ++T) acc[T] = mm<BF>(a[ri][T - Q0 - q], bq[ri][q], acc[T]);
}
DI void put_tile(f4 v, LAS unsigned char* pbase, int T) { *(LAS f4*)(pbase + (T & 3) * 1024) = v; }
template <bool BF, int Q0, int PART> DI void conv_diag(const v4u (&a)[2][8], const v4u (&bq)[2][4], f4 (&acc)[8], LAS unsigned char* pbase) {
    if constexpr (PART == 0) {
        conv_q<BF, Q0, 0, Q0, Q0 + 4>(a, bq, acc);
        SB();
        conv_q<BF, Q0, 1, Q0 + 1, Q0 + 4>(a, bq, acc);
        SB();
        put_tile(acc[Q0], pbase, Q0);
        asm volatile("" : "+v"(acc[Q0 + 1]), "+v"(acc[Q0 + 2]), "+v"(acc[Q0 + 3]));
        SB();
    } else {
        conv_q<BF, Q0, 2, Q0 + 2, Q0 + 4>(a, bq, acc);
        SB();
        put_tile(acc[Q0 + 1], pbase, Q0 + 1);
        acc[Q0 + 3] = mm<BF>(a[0][0], bq[0][3], acc[Q0 + 3]);
        SB();
        put_tile(acc[Q0 + 2], pbase, Q0 + 2);
        acc[Q0 + 3] = mm<BF>(a[1][0], bq[1][3], acc[Q0 + 3]);
        SB();
    }
}
template <bool BF, int PART> DI void conv_off(const v4u (&a)[2][8], const v4u (&bq)[2][4], f4 (&acc)[8]) {
    conv_q<BF, 0, 2 * PART, 4, 8>(a, bq, acc);
    conv_q<BF, 0, 2 * PART + 1, 4, 8>(a, bq, acc);
    asm volatile("" : "+v"(acc[4]), "+v"(acc[5]), "+v"(acc[6]), "+v"(acc[7]));
    SB();
}
DI u32 pk_bf_s(float a, float b) { u32 r; asm("v_cvt_pk_bf16_f32 %0, %1, %2" : "=v"(r) : "v"(a), "v"(b)); return r; }
DI f4 sp4(float a) { return (f4){a, a, a, a}; }

DI f2 cmul(f2 a, f2 b) {
    f2 r;
    asm("v_pk_mul_f32 %0, %1, %2 op_sel:[0,0] op_sel_hi:[0,1]\n\t"
        "v_pk_fma_f32 %0, %1, %2, %0 op_sel:[1,1,0] op_sel_hi:[1,0,1] neg_lo:[0,1,0]" : "=&v"(r) : "v"(a), "v"(b));
    return r;
}
DI f2 cexp_p(float zr2, float fh, float fl, float p) {
    const float mag = __builtin_amdgcn_exp2f(zr2 * p);
    const float an = fmaf(fl, p, __builtin_amdgcn_fractf(fh * p));
    return (f2){__builtin_amdgcn_cosf(an), __builtin_amdgcn_sinf(an)} * sp2(mag);
}
DI float exp_acc(float x) {
    const float L = 1.44269504088896340736f, Ll = 1.92596299112661746e-08f;
    const float h = x * L;
    const float lo = fmaf(x, Ll, fmaf(x, L, -h));
    const float e = __builtin_amdgcn_exp2f(h);
    return fmaf(e * lo, 0.69314718055994530942f, e);
}

template <bool V> struct BC { static constexpr bool v = V; };
#define STAMP(i) do {} while (0)
#define XS(k, e) do {} while (0)
#define XV(k, e, v) do {} while (0)

__global__ void __launch_bounds__(512, 2)
k1_main(const u16* __restrict__ qin, const u16* __restrict__ kin, const u16* __restrict__ vin, const u16* __restrict__ shk,
        const u16* __restrict__ ssmk, const u16* __restrict__ ssmD, const u16* __restrict__ lar, const u16* __restrict__ aim,
        const u16* __restrict__ cre, const u16* __restrict__ cim, const u16* __restrict__ ldt, const u16* __restrict__ dsk,
        u16* __restrict__ out, unsigned char* ws) {
    extern __shared__ __attribute__((aligned(16))) unsigned char smem[];
    LAS unsigned char* L = (LAS unsigned char*)smem;
    const int t = threadIdx.x, l = t & 63;
    const int w = __builtin_amdgcn_readfirstlane(t >> 6);
    const int wg = (blockIdx.x & 7) * 32 + (blockIdx.x >> 3);
    const int H = (wg >> 1) & 1;
    auto body = [&](auto hc, auto wc) __attribute__((always_inline)) {
    constexpr bool HH = decltype(hc)::v, WC = decltype(wc)::v;
    asm volatile("" ::: "memory");
    const int rbk = HH * 128 + (wg >> 2) * 2 + (wg & 1);
    constexpr int c1 = WC;
    const int m = t & 255;
    const int rq = w & 3;
    const int n16 = l & 15, g = l >> 4;
    const __amdgpu_buffer_rsrc_t box = __builtin_amdgcn_make_buffer_rsrc(ws, 0, (int)(WS_HINT + 81920), 0x00020000);
    STAMP(0);
    const v4u sent4 = {SENT, SENT, SENT, SENT};
    const u32 pub_row = (u32)((t >> 2) * 16384 + rbk * 64 + (t & 3) * 16);
    {
        const u32 vo = (u32)l * 16u;
        glds16(kin + (size_t)(8 * rbk + w) * DM, vo, (u32)(L_P + (3 + w) * KV_S));
        glds16(vin + (size_t)(8 * rbk + w) * DM, vo, (u32)(L_P + (11 + w) * KV_S));
        if (!WC && w < 3) {
            if (rbk > 0) glds16(kin + (size_t)(8 * rbk - 3 + w) * DM, vo, (u32)(L_P + w * KV_S));
            else *(LAS v4u*)(L + L_P + w * KV_S + l * 16) = (v4u){0u, 0u, 0u, 0u};
        }
    }
    const int cp = t >> 1, hh = t & 1;
    const v4u sw = *(const v4u*)(shk + cp * 8);
    FENCE();
    const int pch = 2 * wg + ((w >> 1) & 1), pn = t & 127;
    const u32 r_lar = lar[pch * NS + pn], r_aim = aim[pch * NS + pn], r_cre = cre[pch * NS + pn], r_cim = cim[pch * NS + pn];
    const u32 r_cre2 = cre[pch * NS + (pn ^ 64)], r_cim2 = cim[pch * NS + (pn ^ 64)];
    u32 vz; asm volatile("v_mov_b32 %0, 0" : "=v"(vz));
    const u32 r_ldt = ldt[pch + vz];
    const u32 dA2 = *(const u32*)(ssmD + 2 * wg + vz), dB2 = *(const u32*)(dsk + 2 * wg + vz);
    FENCE();
    const int chg = 2 * wg + c1;
    const v4u fcur = *(const v4u*)(ssmk + (size_t)chg * SEQ + 8 * m);
    v2u fprv = *(const v2u*)(ssmk + (size_t)chg * SEQ + (m > 0 ? 8 * m - 4 : 0));
    FENCE();
    const u32 ini_y = (u32)((rbk >> 1) * 16384 + w * 2048 + (rbk & 1) * 1024 + l * 16);
    const u32 ini_u = (u32)((wg >> 1) * 16384 + w * 2048 + (wg & 1) * 1024 + l * 16);
    box_put(sent4, box, ini_y, 1 * MB2);
    FENCE();
    const u32 c_s = now(), r_s = rtime();
    const int row = 8 * rbk + w;
    v2u nonce;
    {
        const unsigned long long id = amd_dispatch_id(), dp = (unsigned long long)__builtin_amdgcn_dispatch_ptr();
        const u32 a = (u32)id * 0x9E3779B1u + (u32)(dp >> 32);
        const u32 b = ((u32)dp ^ ((u32)(id >> 32) * 0x85EBCA77u)) & ~1u;
        nonce = (v2u){a, b | (~a & 1u)};
    }
    const u32 flag_w = t < 8 ? WS_FLAG + (u32)(t * 256 + rbk) * 16 : 0x7FFFFFF0u;
    FENCE();
    if (m < 30) *(LAS v4u*)(L + L_U + c1 * U_S + 16 * m) = (v4u){0u, 0u, 0u, 0u};
    if (m < 8) *(LAS v4u*)(L + L_KC + (c1 * 4 + (m >> 1)) * KC_S + (m & 1) * 16) = (v4u){0u, 0u, 0u, 0u};

    LAS const unsigned char* abase = L + L_KC + (c1 * 4 + (n16 >> 2)) * KC_S + 56 - 16 * g + 8 * (n16 & 3) + 128 * rq;
    LAS const unsigned char* bbase = L + L_U + c1 * U_S + 384 + 32 * n16 + 16 * g - 128 * rq;
    LAS unsigned char* pbase = L + L_P + (rq * 2 + c1) * 4096 + (g * 16 + ((n16 + 4 * g) & 15)) * 16;
    const int eg = l & 3, en = l >> 2;
    LAS const unsigned char* prd = L + L_P + rq * 1024 + (eg * 16 + ((en + 4 * eg) & 15)) * 16;
    LAS const unsigned char* urd = L + L_U + 480 + w * 512 + l * 8;
    LAS unsigned char* ust = L + L_U + (t & 1) * U_S + 480 + (t >> 1) * 16;
    LAS unsigned char* kcw = L + L_KC + c1 * 4 * KC_S + 32 + 16 * m;
    LAS u32* ti = (LAS u32*)(L + L_TI) + (4 * (t & 1)) * 260 + (t >> 1);
    LAS const unsigned char* tr = L + L_TO + ((l & 15) >> 2) * 1088 + (w * 64 + (l >> 4) * 16 + (l & 3) * 4) * 2;
    const u32 pub_col = (u32)((w * 16 + en) * 16384 + wg * 64 + eg * 16);
    const u32 get_p = (u32)((t >> 1) * 64 + (t & 1) * 16);
    const unsigned char* gu = ws + (wg >> 1) * 16384 + (wg & 1) * 32;
    const unsigned char* gy = ws + (rbk >> 1) * 16384 + (rbk & 1) * 32;
    const unsigned char* flag_b = ws + WS_FLAG + (wg >> 5) * 4096;
    const u32 flag_o = (u32)(t >> 1) * 16;
    const u32 hint_w = m < 8 ? WS_HINT + (u32)m * 1024 : 0x7FFFFFF0u;
    const unsigned char* hb = ws + WS_HINT + (wg >> 5) * 1024;
    const u32 ho = (u32)(w * 128 + (l & 7) * 16), ho0 = ho + (u32)(l & 8) * 64;
    const u32 hoy = w == 4 ? (u32)l * 16 : ho, hou = w == 4 ? (u32)(512 + (l & 31) * 16) : ho;

    {
        f2 w0 = {lo2f(sw.x), lo2f(sw.z)}, w1 = {hi2f(sw.x), hi2f(sw.z)}, w2 = {lo2f(sw.y), lo2f(sw.w)}, w3 = {hi2f(sw.y), hi2f(sw.w)};
        asm volatile("" : "+v"(w0), "+v"(w1), "+v"(w2), "+v"(w3));
        lds_bar();
        STAMP(1);
        LAS const unsigned char* kp = L + L_P + 4 * hh * KV_S + cp * 4;
        u32 kr[7], vr[4];
#pragma unroll
        for (int i = 0; i < 7; ++i) kr[i] = *(LAS const u32*)(kp + i * KV_S);
#pragma unroll
        for (int i = 0; i < 4; ++i) vr[i] = *(LAS const u32*)(kp + (11 + i) * KV_S);
        f2 kf[7], kv[4];
#pragma unroll
        for (int i = 0; i < 7; ++i) kf[i] = up2(kr[i]);
#pragma unroll
        for (int r = 0; r < 4; ++r) kv[r] = fma2(kf[r + 3], w0, fma2(kf[r + 2], w1, fma2(kf[r + 1], w2, kf[r] * w3))) * up2(vr[r]);
        const u32 p00 = pk_bf(kv[0].x, kv[1].x), p01 = pk_bf(kv[2].x, kv[3].x);
        const u32 p10 = pk_bf(kv[0].y, kv[1].y), p11 = pk_bf(kv[2].y, kv[3].y);
        const u32 s0 = hh ? p00 : p10, s1 = hh ? p01 : p11;
        const u32 r0 = dppu<0xB1>(s0), r1 = dppu<0xB1>(s1);
        v4u o = hh ? (v4u){r0, r1, p10, p11} : (v4u){p00, p01, r0, r1};
        asm volatile("" : "+v"(o));
            box_put(o, box, pub_row, 0);
        FENCE();
    }
    STAMP(2); STAMP(3);
    f2 C, e128, ea, eb;
    {
        const float dt = rb(exp_acc(bf2f(r_ldt)));
        const float are = rb(exp_acc(bf2f(r_lar)));
        const float zr2 = -(are * dt) * 1.44269504088896340736f;
        const float zi = bf2f(r_aim) * dt;
        const float chi = 0.15915494309189533577f, clo = 6.420638326565253e-09f;
        const float fh = zi * chi;
        const float fl2 = fmaf(zi, clo, fmaf(zi, chi, -fh));
        const u32 am = max(max(r_cre & 0x7FFFu, r_cim & 0x7FFFu), max(r_cre2 & 0x7FFFu, r_cim2 & 0x7FFFu));
        const float mx = bf2f(wave_maxu(am)) * dt;
        int ec = __builtin_amdgcn_frexp_expf(mx);
        ec = ec < -100 ? -100 : ec;
        const float sA = __builtin_amdgcn_ldexpf(1.0f, 13 - ec);
        C = (f2){bf2f(r_cre), -bf2f(r_cim)} * sp2(dt) * sp2(sA);
        e128 = cexp_p(zr2, -fh, -fl2, 128.f);
        ea = cexp_p(zr2, -fh, -fl2, WC ? 1.f : 4.f);
        eb = cexp_p(zr2, -fh, -fl2, WC ? 2.f : 512.f);
        if (l == 0) *(LAS int*)(L + L_KCMP + ((w >> 1) & 1) * KCMP_S + 2824) = ec;
        asm volatile("" : "+v"(C), "+v"(e128), "+v"(ea), "+v"(eb));
        SB();
    }
    STAMP(4);
    {
        if (m == 0) fprv = (v2u){0u, 0u};
        const u32 d[6] = {fprv.x, fprv.y, fcur.x, fcur.y, fcur.z, fcur.w};
        write_copies(kcw, d);
    }
    FENCE();
    v4u fl, q16;
    v4u af[2][8];
    v4u pc;
    {
        LAS unsigned char* tb = L + L_TB + ((w >> 1) & 1) * TB_S + pn * 4;
#define PUT(rowi, v) *(LAS u32*)(tb + (rowi) * TB_R) = pk_h((v).x, (v).y)
#define MIDDLE() do { \
        SB(); \
        vm_wait();                 \
        lds_bar(); \
        if (!WC && w == 0) box_put((v4u){nonce.x, nonce.y, 0u, 0u}, box, flag_w, 0); \
        tfl = now(); \
        box_put(sent4, box, ini_u, 2 * MB2); \
        q16 = *(const v4u*)(qin + (size_t)row * DM + 8 * l); \
        FENCE(); \
        STAMP(5); \
        load_afrags(af, abase); } while (0)
#define MIDDLE2() do { \
        SB(); \
        wait_until(tfl, D_FL + (31 - (blockIdx.x >> 3)) * FL_K); \
        fl = box_req(flag_b, flag_o); \
        FENCE(); } while (0)
        u32 tfl;
        if constexpr (!WC) {
            f2 G = cmul(C, eb);
            f2 e8 = cmul(ea, ea); e8.y = -e8.y;
            f2 B = {16384.f, 0.f};
#pragma unroll
            for (int r = 0; r < 16; ++r) {
                const int h = r + 4;
                if (r == MID_A) { asm volatile("" : "+v"(G), "+v"(B), "+v"(e8)); MIDDLE(); }
                if (r == MID_B) { asm volatile("" : "+v"(G), "+v"(B), "+v"(e8)); MIDDLE2(); }
                PUT(41 + r, B); if (r < 15) B = cmul(B, e8);
                if (h < 8) { PUT(24 + 2 * (h - 4), G); const f2 G4 = cmul(G, ea); PUT(25 + 2 * (h - 4), G4); }
                else if (h <= 16) PUT(24 + h, G);
                if (h < 16) G = cmul(G, e128);
            }
        } else {
            f2 e[8];
            e[1] = ea; e[2] = eb; e[3] = cmul(e[2], e[1]); e[4] = cmul(e[2], e[2]);
            e[5] = cmul(e[4], e[1]); e[6] = cmul(e[4], e[2]); e[7] = cmul(e[4], e[3]);
            f2 G = C;
#pragma unroll
            for (int h = 0; h < 4; ++h) {
                if (h == MID_C) { asm volatile("" : "+v"(G)); MIDDLE(); }
                if (h == MID_D) { asm volatile("" : "+v"(G)); MIDDLE2(); }
                if (h < 2) {
                    PUT(8 * h, G);
#pragma unroll
                    for (int a = 1; a < 8; ++a) { const f2 v = cmul(G, e[a]); PUT(8 * h + a, v); }
                } else {
                    PUT(16 + 4 * (h - 2), G);
#pragma unroll
                    for (int a = 1; a < 4; ++a) { const f2 v = cmul(G, e[2 * a]); PUT(16 + 4 * (h - 2) + a, v); }
                }
                if (h < 3) G = cmul(G, e128);
            }
        }
#undef PUT
    }
    STAMP(6);

    f4 acc[8];
    v4u bq[2][4];
    v4u py, py2;
#define EPILOGUE(BOXI, S0, S1, E) do { \
        f4 pp[8]; \
        _Pragma("unroll") for (int i = 0; i < 8; ++i) pp[i] = *(LAS const f4*)(prd + i * 4096); \
        const f4 y0 = __builtin_elementwise_fma((pp[0] + pp[2]) + (pp[4] + pp[6]), sp4(S0), e0); \
        const f4 y1 = __builtin_elementwise_fma((pp[1] + pp[3]) + (pp[5] + pp[7]), sp4(S1), e1); \
        v4u o; \
        _Pragma("unroll") for (int r = 0; r < 4; ++r) o[r] = clampS(pk_bf_s(y0[r], y1[r])); \
        box_put(o, box, pub_col, (BOXI) * MB2); \
        XS(1, E); \
        FENCE(); \
        __builtin_amdgcn_s_setprio(0); } while (0)
#define ZERO_ACC() do { _Pragma("unroll") for (int i = 0; i < 8; ++i) acc[i] = (f4){0.f, 0.f, 0.f, 0.f}; } while (0)
#define PUT_TI(p, o) do { ti[(o)] = p.x; ti[260 + (o)] = p.y; ti[520 + (o)] = p.z; ti[780 + (o)] = p.w; } while (0)
#define KGEN_MM(ch, mt, bfr) do { \
        LAS const unsigned char* ta = L + L_TB + (ch) * TB_S + (16 * (mt) + n16) * TB_R + 16 * g; \
        v4u afr[8]; \
        _Pragma("unroll") for (int s = 0; s < 8; ++s) afr[s] = *(LAS const v4u*)(ta + 64 * s); \
        f4 d0 = {0.f, 0.f, 0.f, 0.f}, d1 = {0.f, 0.f, 0.f, 0.f}; \
        _Pragma("unroll") for (int s = 0; s < 8; s += 2) { d0 = mm<false>(afr[s], bfr[s], d0); d1 = mm<false>(afr[s + 1], bfr[s + 1], d1); } \
        const f4 dacc = d0 + d1; \
        const int pb = 8 * n16 + ((mt) == 0 ? 4 * (g & 1) + 128 * (g >> 1) : (mt) == 1 ? (g < 2 ? 256 + 128 * g : 256 * g) : 1024 + 512 * g); \
        const int s1 = (mt) == 0 ? 1 : (mt) == 1 ? (g < 2 ? 2 : 4) : 128, s2 = (mt) == 0 ? 2 : (mt) == 1 ? (g < 2 ? 4 : 128) : 256; \
        _Pragma("unroll") for (int i = 0; i < 4; ++i) { \
            int p = pb + (i & 1) * s1 + (i >> 1) * s2; \
            p = p > 2048 ? 2064 : p; \
            u32 ad = __builtin_bit_cast(u32, (float)p) >> 15; \
            if (i == 0) ad = ad > 0x7EFEu ? ad : 0x7EFEu; \
            *(LAS _Float16*)(L + L_KCMP + (ch) * KCMP_S - 0x7EFE + ad) = (_Float16)(dacc[i] * 1.9073486328125e-06f);     \
        } } while (0)
    u32 xd[6];
#define EXPAND1() do { \
        u32* d = xd; \
        const float f0 = (float)(8 * m - 4); \
        const u32 kb = (u32)(L_KCMP + c1 * KCMP_S - 2 * 0x3F7F); \
        _Pragma("unroll") for (int i = 0; i < 6; ++i) { \
            float fa = f0 + (float)(2 * i), fb = f0 + (float)(2 * i + 1); \
            if (i < 3) fa = fmaxf(fa, 0.99609375f); \
            if (i < 2) fb = fmaxf(fb, 0.99609375f); \
            const u32 cd = pk_bf(fa, fb); \
            u32 a0, a1; \
            asm("v_mad_u32_u16 %0, %2, 2, %3\n\tv_mad_u32_u16 %1, %2, 2, %3 op_sel:[1,0,0,0]" : "=&v"(a0), "=v"(a1) : "v"(cd), "s"(kb)); \
            us2 v; v.x = *(LAS const u16*)(L + a0); v.y = *(LAS const u16*)(L + a1); \
            d[i] = __builtin_bit_cast(u32, v); \
        } \
        if (m == 0) { d[0] = 0u; d[1] = 0u; } \
        asm volatile("" : "+v"(d[0]), "+v"(d[1]), "+v"(d[2]), "+v"(d[3]), "+v"(d[4]), "+v"(d[5])); \
        SB(); } while (0)
#define EXPAND2() do { SB(); write_copies(kcw, xd); lds_bar(); SB(); load_afrags(af, abase); SB(); } while (0)
#define TK(c) ((tpc * (u32)(c)) >> 16)
#define HPUT(e, id, val) do { const u32 hv_ = (val); XV(0, e, hv_); __builtin_amdgcn_raw_buffer_store_b32(hv_, box, hint_w, (e) * 8192 + (id) * 4, 16); FENCE(); } while (0)
#define HB(e) (hb + (e) * 8192)
#define DUE(T, n) (~((n) - (T)) >> 31)
    {
        asm volatile("s_waitcnt vmcnt(0)" : "+v"(fl) :: "memory");
#pragma unroll 1
        for (u32 sp = 0; sp < (1u << 16); ++sp) {
            const unsigned long long bad = __builtin_amdgcn_ballot_w64(fl.x != nonce.x || fl.y != nonce.y);
            if (!bad) break;
            unsigned long long sv;
            asm volatile("s_sleep 1\n\ts_mov_b64 %1, exec\n\ts_mov_b64 exec, %4\n\tglobal_load_dwordx4 %0, %2, %3 sc1\n\ts_mov_b64 exec, %1\n\ts_waitcnt vmcnt(0)"
                         : "+v"(fl), "=&s"(sv) : "v"(flag_o), "s"(flag_b), "s"(bad) : "memory");
        }
        asm volatile("" : "+v"(q16));
        pc = box_req(gu, get_p);
        FENCE();
        STAMP(7);
    }
    lds_bar();
#define KGEN2() do { if (w < 6) { \
        const int ch = w >= 3; \
        LAS const unsigned char* tbb = L + L_TB + ch * TB_S + (41 + n16) * TB_R + 16 * g; \
        v4u bfr[8]; \
        _Pragma("unroll") for (int s = 0; s < 8; ++s) bfr[s] = *(LAS const v4u*)(tbb + 64 * s); \
        const int mt = w - 3 * ch; \
        KGEN_MM(ch, mt, bfr); \
    } SB(); } while (0)
    KGEN2();
    u32 tpc;
    {
        const u32 c_n = now(), r_n = rtime();
        const float q = (float)(r_n - r_s) * 65536.f * __builtin_amdgcn_rcpf((float)(c_n - c_s));
        tpc = __builtin_amdgcn_readfirstlane((u32)fminf(fmaxf(q, 2200.f), 4400.f));
    }
    SB();
    f2 x[4] = {up2(q16.x), up2(q16.y), up2(q16.z), up2(q16.w)};
    f2 gate[4];
    v4u hy, hu;
    u32 tq, rq0, nw;
#define LN_Q() do { \
            float rs0, nb0; \
            ln_stats(x, 1.0f, rs0, nb0); \
            _Pragma("unroll") for (int i = 0; i < 4; ++i) gate[i] = fma2(x[i], sp2(rs0), sp2(nb0)); \
            asm volatile("" : "+v"(gate[0]), "+v"(gate[1]), "+v"(gate[2]), "+v"(gate[3])); \
            SB(); } while (0)
#define LNQ_WC LN_Q()
    {
        STAMP(8);
        asm volatile("s_waitcnt vmcnt(0)" : "+v"(pc) :: "memory");
        *(LAS v4u*)ust = pc;
        box_put(sent4, box, ini_y, 3 * MB2);
        lds_bar();
        STAMP(9);
        if constexpr (WC) HPUT(0, wg, rtime() + TK(C_E0A) + T_VIS);
        load_b<0>(bq, bbase);
        const v2u i0 = *(LAS const v2u*)(urd), i1 = *(LAS const v2u*)(urd + U_S);
        ZERO_ACC();
        SB();
        conv_diag<true, 0, 0>(af, bq, acc, pbase);
        conv_diag<true, 0, 1>(af, bq, acc, pbase);
        f4 e0 = (f4){lo2f(i0.x), hi2f(i0.x), lo2f(i0.y), hi2f(i0.y)} * sp4(lo2f(dA2));
        f4 e1 = (f4){lo2f(i1.x), hi2f(i1.x), lo2f(i1.y), hi2f(i1.y)} * sp4(hi2f(dA2));
        asm volatile("" : "+v"(e0), "+v"(e1));
        SB();
        put_tile(acc[3], pbase, 3);
        if constexpr (!WC) {
            __builtin_amdgcn_s_setprio(3);
            lds_bar();
            EPILOGUE(1, 1.0f, 1.0f, 0);
        } else {
            lds_bar();
            HPUT(1, wg, rtime() + TK(C_A1) + T_VIS);
            LNQ_WC;
        }
        STAMP(10);
        v4u b2[2][4];
        conv_off<true, 0>(af, bq, acc);
        box_put(sent4, box, ini_u, 4 * MB2);
        FENCE();
        if constexpr (!HH && !WC) { hy = hint_req(HB(0), ho0); FENCE(); }
        load_b<4>(b2, bbase);
        conv_off<true, 1>(af, bq, acc);
        box_put(sent4, box, ini_y, 5 * MB2);
        FENCE();
        lds_bar();
        STAMP(11);
        if constexpr (WC && !HH) HPUT(2, rbk, rtime() + TK(C_UA) + T_VIS);
#define Y0_EARLY(BOXI) do { \
            tq = hint_chk((BOXI) == 1, hy, rq0); \
            nw = DUE(tq, rq0); \
            py = sent4; py2 = sent4; \
            box_req_if(py, gy + (BOXI) * MB2, get_p, nw); box_req_if(py2, gy + (BOXI) * MB2 + 8192, get_p, nw); \
            SB(); } while (0)
#define Y0_LATE(E, BOXI) do { \
            if (!nw) hint_wait(tq, rq0, HB(E), ho0 RM(1u << (16 + (E)))); \
            box_req_if(py, gy + (BOXI) * MB2, get_p, nw ^ 1); box_req_if(py2, gy + (BOXI) * MB2 + 8192, get_p, nw ^ 1); \
            XV(2, E, nw ? rq0 : rtime()); \
            SB(); } while (0)
        conv_diag<true, 4, 0>(af, b2, acc, pbase);
        if constexpr (!HH && !WC) Y0_EARLY(1);
        conv_diag<true, 4, 1>(af, b2, acc, pbase);
        if constexpr (HH) { hy = hint_req(HB(1), hoy); FENCE(); }
        put_tile(acc[7], pbase, 7);
        if constexpr (WC) __builtin_amdgcn_s_setprio(3);
        lds_bar();
        if constexpr (WC) EPILOGUE(1, 1.0f, 1.0f, 1);
        else { if constexpr (!HH) Y0_LATE(0, 1); LN_Q(); }
        STAMP(12);
    }
    const float sc0 = __builtin_amdgcn_ldexpf(1.0f / USCALE, *(LAS const int*)(L + L_KCMP + 2824) - 7);
    const float sc1 = __builtin_amdgcn_ldexpf(1.0f / USCALE, *(LAS const int*)(L + L_KCMP + KCMP_S + 2824) - 7);

    v4u yAp;
#pragma unroll
    for (int it = 0; it < 3; ++it) {
        const int CR = it ? C_R1 : C_R0;
        const unsigned char* gub = gu + (2 + 2 * it) * MB2;
        {
            if (it == 1) {
                gate[0] = up2(yAp.x); gate[1] = up2(yAp.y); gate[2] = up2(yAp.z); gate[3] = up2(yAp.w);
                asm volatile("" : "+v"(gate[0]), "+v"(gate[1]), "+v"(gate[2]), "+v"(gate[3]));
            }
            if constexpr (HH) {
                if (it == 0) EXPAND1();
                tq = hint_chk(WC, hy, nw);
                hint_wait(tq, nw, HB(1 + 4 * it), hoy RM(1u << (17 + 4 * it)));
                py = box_req(gy + (1 + 2 * it) * MB2, get_p);
                XS(2, 1 + 4 * it);
                if (it < 2) {
                    if constexpr (WC) HPUT(3 + 4 * it, rbk, rtime() + T_RD + TK(CR) + T_VIS);
                    else { hu = hint_req(HB(2 + 4 * it), ho); pc = sent4; }
                }
                if (it == 0) EXPAND2();
                py = box_fin(it < 2, py, gy + (1 + 2 * it) * MB2, get_p, 1 + 4 * it);
                XS(3, 1 + 4 * it);
                PUT_TI(py, 0);
            } else if constexpr (!WC) {
                py = box_fin(1, py, gy + (1 + 2 * it) * MB2, get_p, 4 * it);
                PUT_TI(py, 0);
                py2 = box_fin(0, py2, gy + (1 + 2 * it) * MB2 + 8192, get_p, 4 * it);
                XS(3, 4 * it);
                PUT_TI(py2, 128);
            } else if (it == 0) {
                asm volatile("s_waitcnt vmcnt(5)" ::: "memory");
            }
            lds_bar();
            STAMP(13 + 7 * it);
            const v4u yv = *(LAS const v4u*)(L + L_TI + w * 1040 + l * 16);
            SB();
            if (it < 2) {
                if constexpr (HH && !WC) {
                    tq = hint_chk(0, hu, rq0);
                    nw = DUE(tq, rq0);
                    box_req_if(pc, gub, get_p, nw);
                    SB();
                }
            }
#define U0_TRY() do { \
                    u32 c = 0; \
                    if (!nw) { rq0 = rtime(); c = DUE(tq, rq0); } \
                    box_req_if(pc, gub, get_p, c); \
                    nw |= c; SB(); } while (0)
            const f2 yc[4] = {up2(yv.x), up2(yv.y), up2(yv.z), up2(yv.w)};
            float rs, nb;
            if (it == 0) {
#pragma unroll
                for (int i = 0; i < 4; ++i) x[i] = fma2(gate[i], yc[i], x[i]);
                yAp = yv;
            } else {
#pragma unroll
                for (int i = 0; i < 4; ++i) x[i] += yc[i];
            }
            if (it == 2) {
                const __amdgpu_buffer_rsrc_t ob = __builtin_amdgcn_make_buffer_rsrc(out, 0, SEQ * DM * 2, 0x00020000);
                v4u o = {pk_bf(x[0].x, x[0].y), pk_bf(x[1].x, x[1].y), pk_bf(x[2].x, x[2].y), pk_bf(x[3].x, x[3].y)};
                __builtin_amdgcn_raw_buffer_store_b128(o, ob, (u32)(row * DM * 2 + 16 * l), 0, 17);
                STAMP(14 + 7 * it);
                break;
            }
            if (it == 1) {
                ln_stats(x, 1.0f, rs, nb);
                if constexpr (HH && !WC) { SB(); U0_TRY(); }
#pragma unroll
                for (int i = 0; i < 4; ++i) x[i] = fma2(fma2(x[i], sp2(rs), sp2(nb)), gate[i], x[i]);
            }
            ln_stats(x, USCALE, rs, nb);
            v4u uo;
#pragma unroll
            for (int i = 0; i < 4; ++i) { const f2 u = fma2(x[i], sp2(rs), sp2(nb)); uo[i] = pk_h(u.x, u.y); }
            *(LAS v4u*)(L + L_TO + w * 1088 + l * 16) = uo;
            if constexpr (!WC) {
                SB();
                if constexpr (HH) U0_TRY();
                else hu = hint_req(HB(2 + 4 * it), ho);
                SB();
            } else if constexpr (HH) { SB(); hu = hint_req(HB(3 + 4 * it), hou); SB(); }
            lds_bar();
            const v2u lo2 = tr_read(tr), hi2 = tr_read(tr + 4 * 1088);
            box_put((v4u){clampS(lo2.x), clampS(lo2.y), clampS(hi2.x), clampS(hi2.y)}, box, pub_row, (2 + 2 * it) * MB2);
            FENCE();
            XS(1, 2 + 4 * it + HH);
            STAMP(14 + 7 * it);
        }
        {
            if constexpr (HH) {
                if constexpr (!WC) {
                    if (!nw) hint_wait(tq, rq0, HB(2 + 4 * it), ho RM(1u << (18 + 4 * it)));
                    box_req_if(pc, gub, get_p, nw ^ 1);
                    XV(2, 2 + 4 * it, nw ? rq0 : rtime());
                    if (!nw) vm_wait();
                    pc = box_fin(1, pc, gub, get_p, 2 + 4 * it);
                    XS(3, 2 + 4 * it);
                    *(LAS v4u*)ust = pc;
                }
            } else {
                if constexpr (WC) rq0 = rtime();
                if (it == 0) EXPAND1();
                if constexpr (!WC) {
                    tq = hint_chk(1, hu, nw);
                    hint_wait(tq, nw, HB(2 + 4 * it), ho RM(1u << (18 + 4 * it)));
                    pc = box_req(gub, get_p);
                    XS(2, 2 + 4 * it);
                }
                if (it == 0) EXPAND2();
                if constexpr (!WC) { pc = box_fin(0, pc, gub, get_p, 2 + 4 * it); XS(3, 2 + 4 * it); *(LAS v4u*)ust = pc; }
                else { wait_rt(rq0 + T_H1); tq = hint_chk(0, hint_req(HB(3 + 4 * it), hou), nw); }
            }
            lds_bar();
            STAMP(15 + 7 * it);
            if constexpr (WC) { rq0 = rtime(); HPUT(4 + 4 * it, wg, rq0 + TK(C_E0) + T_VIS); }
            load_b<0>(bq, bbase);
            const float d0 = lo2f(dB2) * (1.0f / USCALE), d1 = hi2f(dB2) * (1.0f / USCALE);
#define SKIP_LOAD() const v2u i0 = *(LAS const v2u*)(urd), i1 = *(LAS const v2u*)(urd + U_S)
#define SKIP_TERMS() \
            const h2 h00 = as_h2(i0.x), h01 = as_h2(i0.y), h10 = as_h2(i1.x), h11 = as_h2(i1.y); \
            f4 e0 = (f4){(float)h00.x, (float)h00.y, (float)h01.x, (float)h01.y} * sp4(d0); \
            f4 e1 = (f4){(float)h10.x, (float)h10.y, (float)h11.x, (float)h11.y} * sp4(d1); \
            asm volatile("" : "+v"(e0), "+v"(e1)); SB()
            ZERO_ACC();
            if constexpr (!WC) {
                SKIP_LOAD();
                SB();
                conv_diag<false, 0, 0>(af, bq, acc, pbase);
                conv_diag<false, 0, 1>(af, bq, acc, pbase);
                SKIP_TERMS();
                put_tile(acc[3], pbase, 3);
                __builtin_amdgcn_s_setprio(3);
                lds_bar();
                STAMP(16 + 7 * it);
                EPILOGUE(3 + 2 * it, sc0, sc1, 4 + 4 * it);
            } else {
                SB();
                conv_diag<false, 0, 0>(af, bq, acc, pbase);
                if constexpr (HH) { tq = hint_chk(2, hu, nw); SB(); }
                conv_diag<false, 0, 1>(af, bq, acc, pbase);
                put_tile(acc[3], pbase, 3);
                lds_bar();
                STAMP(16 + 7 * it);
                hint_wait(tq, nw, HB(3 + 4 * it), hou RM(1u << (19 + 4 * it)));
                pc = box_req(gub, get_p);
                XS(2, 3 + 4 * it);
                rq0 += TK(C_01);
                HPUT(5 + 4 * it, wg, rq0 + max(0, (int)(rtime() + T_RD - rq0)) + TK(C_E1) + T_VIS);
            }
            if (it == 0 && !WC) {
                if (w == 0) __builtin_amdgcn_raw_buffer_store_b64(~nonce, box, flag_w, 0, 16);
                FENCE();
            }
            conv_off<false, 0>(af, bq, acc);
            if constexpr (!HH && !WC) { hy = hint_req(HB(4 + 4 * it), ho0); FENCE(); }
            conv_off<false, 1>(af, bq, acc);
            if constexpr (WC) {
                pc = box_fin(1, pc, gub, get_p, 3 + 4 * it);
                XS(3, 3 + 4 * it);
                *(LAS v4u*)ust = pc;
            }
            STAMP(17 + 7 * it);
            lds_bar();
            STAMP(18 + 7 * it);
            if (WC && !HH && it == 0) HPUT(6, rbk, rtime() + TK(C_UB) + T_VIS);
            load_b<4>(bq, bbase);
            if constexpr (WC) {
                SKIP_LOAD();
                SB();
                conv_diag<false, 4, 0>(af, bq, acc, pbase);
                conv_diag<false, 4, 1>(af, bq, acc, pbase);
                SKIP_TERMS();
                if constexpr (HH) { hy = hint_req(HB(5 + 4 * it), hoy); FENCE(); }
                put_tile(acc[7], pbase, 7);
                __builtin_amdgcn_s_setprio(3);
                lds_bar();
                EPILOGUE(3 + 2 * it, sc0, sc1, 5 + 4 * it);
            } else {
                conv_diag<false, 4, 0>(af, bq, acc, pbase);
                if constexpr (!HH) Y0_EARLY(3 + 2 * it);
                conv_diag<false, 4, 1>(af, bq, acc, pbase);
                if constexpr (HH) { hy = hint_req(HB(5 + 4 * it), hoy); FENCE(); }
                put_tile(acc[7], pbase, 7);
                lds_bar();
                if constexpr (!HH) Y0_LATE(4 + 4 * it, 3 + 2 * it);
            }
            STAMP(19 + 7 * it);
        }
    }
    };
    if (H) { if (w < 4) body(BC<true>{}, BC<false>{}); else body(BC<true>{}, BC<true>{}); }
    else   { if (w < 4) body(BC<false>{}, BC<false>{}); else body(BC<false>{}, BC<true>{}); }
}

extern "C" void kernel_launch(void* const* d_in, const int* in_sizes, int n_in, void* d_out, int out_size, void* d_ws, size_t ws_size,
                              hipStream_t stream) {
    (void)hipFuncSetAttribute((const void*)k1_main, hipFuncAttributeMaxDynamicSharedMemorySize, L_END);
    k1_main<<<256, 512, L_END, stream>>>((const u16*)d_in[0], (const u16*)d_in[1], (const u16*)d_in[2], (const u16*)d_in[3], (const u16*)d_in[4],
                                         (const u16*)d_in[5], (const u16*)d_in[7], (const u16*)d_in[8], (const u16*)d_in[9], (const u16*)d_in[10],
                                         (const u16*)d_in[11], (const u16*)d_in[12], (u16*)d_out, (unsigned char*)d_ws);
}
```

```cpp
#include <hip/hip_runtime.h>

typedef unsigned short u16;
typedef unsigned u32;
typedef u32 v4u __attribute__((ext_vector_type(4)));
typedef u32 v2u __attribute__((ext_vector_type(2)));
typedef float f4 __attribute__((ext_vector_type(4)));
typedef float f2 __attribute__((ext_vector_type(2)));
typedef __bf16 bf8 __attribute__((ext_vector_type(8)));
typedef __bf16 bf2 __attribute__((ext_vector_type(2)));
typedef _Float16 h8 __attribute__((ext_vector_type(8)));
typedef _Float16 h2 __attribute__((ext_vector_type(2)));
typedef short s4 __attribute__((ext_vector_type(4)));
typedef unsigned short us2 __attribute__((ext_vector_type(2)));
#define LAS __attribute__((address_space(3)))
#define DI __device__ __forceinline__
extern "C" __device__ unsigned long long amd_dispatch_id() __asm("llvm.amdgcn.dispatch.id");

#define LDP "sc1"
#define STA 16
constexpr int SEQ = 2048, DM = 512, NS = 128;
constexpr u32 SENT = 0xFFFFFFFFu;
constexpr u32 MB2 = 2u * 1024u * 1024u;
constexpr u32 WS_FLAG = 6 * MB2;
constexpr size_t WS_STAMP = 8 * (size_t)MB2;
constexpr float USCALE = 16.0f;
constexpr u32 WS_HINT = WS_FLAG + 32768;
#define D_FL 700
#define MID_A 0
#define MID_B 8
#define MID_C 0
#define MID_D 2
#define FL_K 25
#define T_VIS 30
#define T_RD 48
#define T_H1 35
#define E0_SL 0
#define C_E0A 1650
#define C_E0 1850
#define C_A1 3400
#define C_01 2600
#define C_E1 1450
#define C_R0 1250
#define C_R1 1400
#define C_UA 3300
#define C_UB 3300
#define CODE_B 65536
#define PF_W 7
constexpr u32 HW = 800;

constexpr int KC_S = 4160;
constexpr int L_KC = 0;
constexpr int U_S = 4672;
constexpr int L_U = L_KC + 2 * 4 * KC_S;
constexpr int L_TI = L_U + 2 * U_S;
constexpr int L_TO = L_TI + 8 * 1040;
constexpr int KCMP_S = 2832;
constexpr int L_KCMP = L_TO + 8 * 1088;
constexpr int TB_R = 544, TB_S = 57 * TB_R;
constexpr int L_TB = L_KCMP + 2 * KCMP_S;
constexpr int L_P = L_TB + 2 * TB_S;
constexpr int KV_S = 1056;
constexpr int L_END = L_P + 32768;

DI float bf2f(u32 h) { return __builtin_bit_cast(float, h << 16); }
DI float lo2f(u32 d) { return __builtin_bit_cast(float, d << 16); }
DI float hi2f(u32 d) { return __builtin_bit_cast(float, d & 0xFFFF0000u); }
DI f2 up2(u32 d) { return (f2){lo2f(d), hi2f(d)}; }
DI float rb(float x) { f2 t = {0.f, x}; return __builtin_bit_cast(float, __builtin_convertvector(t, bf2)); }
DI u32 pk_bf(float a, float b) { f2 t = {a, b}; return __builtin_bit_cast(u32, __builtin_convertvector(t, bf2)); }
DI u32 pk_h(float a, float b) { f2 t = {a, b}; return __builtin_bit_cast(u32, __builtin_convertvector(t, h2)); }
DI h2 as_h2(u32 d) { return __builtin_bit_cast(h2, d); }
DI u32 clampS(u32 d) { return d < 0xFFFFFFFEu ? d : 0xFFFFFFFEu; }
DI f2 fma2(f2 a, f2 b, f2 c) { return __builtin_elementwise_fma(a, b, c); }
DI f2 sp2(float a) { return (f2){a, a}; }
template <int CTRL, int RM = 0xf> DI float dppf(float v) {
    return __builtin_bit_cast(float, __builtin_amdgcn_update_dpp(0, __builtin_bit_cast(int, v), CTRL, RM, 0xf, true));
}
DI float rdl(float v, int l) { return __builtin_bit_cast(float, __builtin_amdgcn_readlane(__builtin_bit_cast(int, v), l)); }
DI void wave_sum2(float& a, float& b) {
    const auto sw = __builtin_amdgcn_permlane32_swap(__builtin_bit_cast(u32, a), __builtin_bit_cast(u32, b), false, false);
    float r = __builtin_bit_cast(float, (u32)sw[0]) + __builtin_bit_cast(float, (u32)sw[1]);
    r += dppf<0xB1>(r); r += dppf<0x4E>(r); r += dppf<0x141>(r); r += dppf<0x140>(r);
    asm volatile("s_nop 1\n\tv_add_f32_dpp %0, %0, %0 row_bcast:15 row_mask:0xa bank_mask:0xf\n\ts_nop 0" : "+v"(r));
    a = rdl(r, 31); b = rdl(r, 63);
}
template <int CTRL, int RM = 0xf> DI u32 dppu(u32 v) { return (u32)__builtin_amdgcn_update_dpp(0, (int)v, CTRL, RM, 0xf, true); }
DI u32 wave_maxu(u32 v) {
    v = max(v, dppu<0xB1>(v)); v = max(v, dppu<0x4E>(v)); v = max(v, dppu<0x141>(v)); v = max(v, dppu<0x140>(v));
    return max(max((u32)__builtin_amdgcn_readlane(v, 0), (u32)__builtin_amdgcn_readlane(v, 16)),
               max((u32)__builtin_amdgcn_readlane(v, 32), (u32)__builtin_amdgcn_readlane(v, 48)));
}
DI void ln_stats(const f2* x, float scale, float& rs, float& nb) {
    const float c = rdl(x[0].x, 0);
    f2 d[4];
#pragma unroll
    for (int i = 0; i < 4; ++i) d[i] = x[i] - sp2(c);
    const f2 s2 = (d[0] + d[1]) + (d[2] + d[3]);
    const f2 q2 = fma2(d[3], d[3], fma2(d[2], d[2], fma2(d[1], d[1], d[0] * d[0])));
    float s = s2.x + s2.y, q = q2.x + q2.y;
    wave_sum2(s, q);
    const float md = s * (1.0f / DM);
    const float var = fmaxf(fmaf(-md, md, q * (1.0f / DM)), 0.f);
    const float mb = rb(c + md);
    const float sd = rb(__builtin_amdgcn_sqrtf(rb(rb(var) + 1e-5f)));
    rs = __builtin_amdgcn_rcpf(sd) * scale;
    nb = -mb * rs;
}

DI bool has_sent(v4u v) { const u32 a = v.x > v.y ? v.x : v.y, b = v.z > v.w ? v.z : v.w; return (a > b ? a : b) == SENT; }
#define SB() __builtin_amdgcn_sched_barrier(0)
#define FENCE() do { asm volatile("" ::: "memory"); SB(); } while (0)
DI u32 now() { unsigned long long t_; asm volatile("s_memtime %0\n\ts_waitcnt lgkmcnt(0)" : "=s"(t_) :: "memory"); return (u32)t_; }
DI void wait_until(u32 t0, u32 d) {
#pragma unroll 1
    for (int i = 0; i < 512; ++i) if ((int)(now() - t0 - d) >= 0) break;
}
DI u32 rtime() { unsigned long long t_; asm volatile("s_memrealtime %0\n\ts_waitcnt lgkmcnt(0)" : "=s"(t_) :: "memory"); return (u32)t_; }
DI void wait_rt(u32 T) {
#pragma unroll 1
    for (int i = 0; i < 1024; ++i) if ((int)(rtime() - T) >= 0) break;
}
DI v4u box_req(const unsigned char* base, u32 off) {
    v4u v = {SENT, SENT, SENT, SENT};
    asm volatile("global_load_dwordx4 %0, %1, %2 " LDP : "+v"(v) : "v"(off), "s"(base) : "memory"); return v;
}
DI void box_req_if(v4u& v, const unsigned char* base, u32 off, u32 c) {
    asm volatile("s_cmp_eq_u32 %3, 0\n\ts_cbranch_scc1 .Lrq%=\n\tglobal_load_dwordx4 %0, %1, %2 " LDP "\n.Lrq%=:" : "+v"(v) : "v"(off), "s"(base), "s"(__builtin_amdgcn_readfirstlane(c)) : "memory", "scc");
}
DI v4u hint_req(const unsigned char* base, u32 off) {
    v4u v; asm volatile("global_load_dwordx4 %0, %1, %2 " LDP : "=v"(v) : "v"(off), "s"(base) : "memory"); return v;
}
DI u32 hint_chk(int N, v4u h, u32& n) {
    asm volatile("s_waitcnt vmcnt(%1)" : "+v"(h) : "i"(N) : "memory");
    n = rtime();
    const u32 b = n - HW;
    const u32 mx = wave_maxu(max(max(h.x - b, h.y - b), max(h.z - b, h.w - b)));
    return mx <= 2 * HW ? b + mx : n + 0x40000000u;
}
#define RM(x)
#define RMP
#define RMS() do {} while (0)
DI void hint_wait(u32 T, u32 n, const unsigned char* base, u32 off RMP) {
#pragma unroll 1
    for (int i = 0; i < 6 && (int)(T - n) > (int)(2 * HW); ++i) {
        RMS();
        T = hint_chk(0, hint_req(base, off), n);
    }
    if ((int)(T - n) <= (int)(2 * HW)) wait_rt(T);
}
#define box_fin(N, v, base, off, id) box_fin_(N, v, base, off RM(1u << (id)))
DI v4u box_fin_(int N, v4u v, const unsigned char* base, u32 off RMP) {
    asm volatile("s_waitcnt vmcnt(%1)" : "+v"(v) : "i"(N) : "memory");
#pragma unroll 1
    for (u32 sp = 0; sp < (1u << 16); ++sp) {
        const unsigned long long bad = __builtin_amdgcn_ballot_w64(has_sent(v));
        if (!bad) break;
        RMS();
        unsigned long long sv;
        asm volatile("s_sleep 2\n\ts_mov_b64 %1, exec\n\ts_mov_b64 exec, %4\n\tglobal_load_dwordx4 %0, %2, %3 " LDP "\n\ts_mov_b64 exec, %1\n\ts_waitcnt vmcnt(0)"
                     : "+v"(v), "=&s"(sv) : "v"(off), "s"(base), "s"(bad) : "memory");
    }
    return v;
}
DI void box_put(v4u v, __amdgpu_buffer_rsrc_t r, u32 off, u32 soff) {
    asm volatile("" : "+v"(v));
    __builtin_amdgcn_raw_buffer_store_b128(v, r, off, soff, STA);
    asm volatile("s_nop 1" :: "v"(v) : "memory");
}
DI void glds16(const void* base, u32 off, u32 lds) {
    asm volatile("s_mov_b32 m0, %2\n\ts_nop 0\n\tglobal_load_lds_dwordx4 %0, %1" :: "v"(off), "s"(base), "s"(lds) : "memory");
}
DI void lds_bar() { asm volatile("s_waitcnt lgkmcnt(0)\n\ts_barrier" ::: "memory"); }
DI void vm_wait() { asm volatile("s_waitcnt vmcnt(0)" ::: "memory"); }

DI void write_copies(LAS unsigned char* kc  , const u32* d) {
    u32 o[5];
#pragma unroll
    for (int i = 0; i < 5; ++i) o[i] = __builtin_amdgcn_alignbit(d[i + 1], d[i], 16);
    *(LAS v4u*)(kc) = (v4u){d[2], d[3], d[4], d[5]};
    *(LAS v4u*)(kc + KC_S) = (v4u){o[1], o[2], o[3], o[4]};
    *(LAS v4u*)(kc + 2 * KC_S) = (v4u){d[1], d[2], d[3], d[4]};
    *(LAS v4u*)(kc + 3 * KC_S) = (v4u){o[0], o[1], o[2], o[3]};
}

template <bool BF> DI f4 mm(v4u a, v4u b, f4 c) {
    if constexpr (BF) return __builtin_amdgcn_mfma_f32_16x16x32_bf16(__builtin_bit_cast(bf8, a), __builtin_bit_cast(bf8, b), c, 0, 0, 0);
    else return __builtin_amdgcn_mfma_f32_16x16x32_f16(__builtin_bit_cast(h8, a), __builtin_bit_cast(h8, b), c, 0, 0, 0);
}
DI v2u tr_read(LAS const unsigned char* p) { return __builtin_bit_cast(v2u, __builtin_amdgcn_ds_read_tr16_b64_v4i16((LAS s4*)p)); }

template <int P0 = 0, int P1 = 8> DI void load_afrags(v4u (&a)[2][8], LAS const unsigned char* abase) {
#pragma unroll
    for (int p = P0; p < P1; ++p)
#pragma unroll
        for (int ri = 0; ri < 2; ++ri) {
            const v2u lo = tr_read(abase + 512 * p + 64 * ri + 8), hi = tr_read(abase + 512 * p + 64 * ri);
            a[ri][p] = (v4u){lo.x, lo.y, hi.x, hi.y};
        }
}
template <int Q0> DI void load_b(v4u (&bq)[2][4], LAS const unsigned char* bbase) {
#pragma unroll
    for (int q = 0; q < 4; ++q) {
#pragma unroll
        for (int ri = 0; ri < 2; ++ri) { bq[ri][q] = *(LAS const v4u*)(bbase + 512 * (Q0 + q) + 64 * (1 - ri)); SB(); }
    }
}
template <bool BF, int Q0, int q, int TA, int TB> DI void conv_q(const v4u (&a)[2][8], const v4u (&bq)[2][4], f4 (&acc)[8]) {
#pragma unroll
    for (int ri = 0; ri < 2; ++ri)
#pragma unroll
        for (int T = TA; T < TB; ++T) acc[T] = mm<BF>(a[ri][T - Q0 - q], bq[ri][q], acc[T]);
}
DI void put_tile(f4 v, LAS unsigned char* pbase, int T) { *(LAS f4*)(pbase + (T & 3) * 1024) = v; }
template <bool BF, int Q0, int PART> DI void conv_diag(const v4u (&a)[2][8], const v4u (&bq)[2][4], f4 (&acc)[8], LAS unsigned char* pbase) {
    if constexpr (PART == 0) {
        conv_q<BF, Q0, 0, Q0, Q0 + 4>(a, bq, acc);
        SB();
        conv_q<BF, Q0, 1, Q0 + 1, Q0 + 4>(a, bq, acc);
        SB();
        put_tile(acc[Q0], pbase, Q0);
        asm volatile("" : "+v"(acc[Q0 + 1]), "+v"(acc[Q0 + 2]), "+v"(acc[Q0 + 3]));
        SB();
    } else {
        conv_q<BF, Q0, 2, Q0 + 2, Q0 + 4>(a, bq, acc);
        SB();
        put_tile(acc[Q0 + 1], pbase, Q0 + 1);
        acc[Q0 + 3] = mm<BF>(a[0][0], bq[0][3], acc[Q0 + 3]);
        SB();
        put_tile(acc[Q0 + 2], pbase, Q0 + 2);
        acc[Q0 + 3] = mm<BF>(a[1][0], bq[1][3], acc[Q0 + 3]);
        SB();
    }
}
template <bool BF, int PART> DI void conv_off(const v4u (&a)[2][8], const v4u (&bq)[2][4], f4 (&acc)[8]) {
    conv_q<BF, 0, 2 * PART, 4, 8>(a, bq, acc);
    conv_q<BF, 0, 2 * PART + 1, 4, 8>(a, bq, acc);
    asm volatile("" : "+v"(acc[4]), "+v"(acc[5]), "+v"(acc[6]), "+v"(acc[7]));
    SB();
}
DI u32 pk_bf_s(float a, float b) { u32 r; asm("v_cvt_pk_bf16_f32 %0, %1, %2" : "=v"(r) : "v"(a), "v"(b)); return r; }
DI f4 sp4(float a) { return (f4){a, a, a, a}; }

DI f2 cmul(f2 a, f2 b) {
    f2 r;
    asm("v_pk_mul_f32 %0, %1, %2 op_sel:[0,0] op_sel_hi:[0,1]\n\t"
        "v_pk_fma_f32 %0, %1, %2, %0 op_sel:[1,1,0] op_sel_hi:[1,0,1] neg_lo:[0,1,0]" : "=&v"(r) : "v"(a), "v"(b));
    return r;
}
DI f2 cexp_p(float zr2, float fh, float fl, float p) {
    const float mag = __builtin_amdgcn_exp2f(zr2 * p);
    const float an = fmaf(fl, p, __builtin_amdgcn_fractf(fh * p));
    return (f2){__builtin_amdgcn_cosf(an), __builtin_amdgcn_sinf(an)} * sp2(mag);
}
DI float exp_acc(float x) {
    const float L = 1.44269504088896340736f, Ll = 1.92596299112661746e-08f;
    const float h = x * L;
    const float lo = fmaf(x, Ll, fmaf(x, L, -h));
    const float e = __builtin_amdgcn_exp2f(h);
    return fmaf(e * lo, 0.69314718055994530942f, e);
}

template <bool V> struct BC { static constexpr bool v = V; };
#define STAMP(i) do {} while (0)
#define XS(k, e) do {} while (0)
#define XV(k, e, v) do {} while (0)

__global__ void __launch_bounds__(512, 2)
k1_main(const u16* __restrict__ qin, const u16* __restrict__ kin, const u16* __restrict__ vin, const u16* __restrict__ shk,
        const u16* __restrict__ ssmk, const u16* __restrict__ ssmD, const u16* __restrict__ lar, const u16* __restrict__ aim,
        const u16* __restrict__ cre, const u16* __restrict__ cim, const u16* __restrict__ ldt, const u16* __restrict__ dsk,
        u16* __restrict__ out, unsigned char* ws) {
    extern __shared__ __attribute__((aligned(16))) unsigned char smem[];
    LAS unsigned char* L = (LAS unsigned char*)smem;
    const int t = threadIdx.x, l = t & 63;
    const int w = __builtin_amdgcn_readfirstlane(t >> 6);
    const int wg = (blockIdx.x & 7) * 32 + (blockIdx.x >> 3);
    const int H = (wg >> 1) & 1;
    const int rbk = H * 128 + (wg >> 2) * 2 + (wg & 1);
    const int c1 = w >> 2;
    const int m = t & 255;
    const int rq = w & 3;
    const int n16 = l & 15, g = l >> 4;
    const __amdgpu_buffer_rsrc_t box = __builtin_amdgcn_make_buffer_rsrc(ws, 0, (int)(WS_HINT + 81920), 0x00020000);
    STAMP(0);
    u32 pfd;
    {
        unsigned long long pc;
        asm volatile("s_getpc_b64 %0" : "=s"(pc));
        const u32 po = min((u32)((l * 16 + ((blockIdx.x >> 3) & 15)) * 128), (u32)(CODE_B - 4));
        if (w == PF_W) asm volatile("global_load_dword %0, %1, %2" : "=v"(pfd) : "v"(po), "s"(pc) : "memory");
        else asm volatile("" : "=v"(pfd));
    }
    const v4u sent4 = {SENT, SENT, SENT, SENT};
    const u32 pub_row = (u32)((t >> 2) * 16384 + rbk * 64 + (t & 3) * 16);
    {
        const u32 vo = (u32)l * 16u;
        glds16(kin + (size_t)(8 * rbk + w) * DM, vo, (u32)(L_P + (3 + w) * KV_S));
        glds16(vin + (size_t)(8 * rbk + w) * DM, vo, (u32)(L_P + (11 + w) * KV_S));
        if (w < 3) {
            glds16(kin + (size_t)max(8 * rbk - 3 + w, 0) * DM, vo, (u32)(L_P + w * KV_S));
        }
    }
    const int cp = t >> 1, hh = t & 1;
    const v4u sw = *(const v4u*)(shk + cp * 8);
    FENCE();
    const int pch = 2 * wg + ((w >> 1) & 1), pn = t & 127;
    const u32 r_lar = lar[pch * NS + pn], r_aim = aim[pch * NS + pn], r_cre = cre[pch * NS + pn], r_cim = cim[pch * NS + pn];
    const u32 r_cre2 = cre[pch * NS + (pn ^ 64)], r_cim2 = cim[pch * NS + (pn ^ 64)];
    u32 vz; asm volatile("v_mov_b32 %0, 0" : "=v"(vz));
    const u32 r_ldt = ldt[pch + vz];
    const u32 dA2 = *(const u32*)(ssmD + 2 * wg + vz), dB2 = *(const u32*)(dsk + 2 * wg + vz);
    FENCE();
    const int chg = 2 * wg + c1;
    const v4u fcur = *(const v4u*)(ssmk + (size_t)chg * SEQ + 8 * m);
    v2u fprv = *(const v2u*)(ssmk + (size_t)chg * SEQ + (m > 0 ? 8 * m - 4 : 0));
    FENCE();
    const u32 ini_y = (u32)((rbk >> 1) * 16384 + w * 2048 + (rbk & 1) * 1024 + l * 16);
    const u32 ini_u = (u32)((wg >> 1) * 16384 + w * 2048 + (wg & 1) * 1024 + l * 16);
    box_put(sent4, box, ini_y, 1 * MB2);
    FENCE();
    const u32 c_s = now(), r_s = rtime();
    const int row = 8 * rbk + w;
    v2u nonce;
    {
        const unsigned long long id = amd_dispatch_id(), dp = (unsigned long long)__builtin_amdgcn_dispatch_ptr();
        const u32 a = (u32)id * 0x9E3779B1u + (u32)(dp >> 32);
        const u32 b = ((u32)dp ^ ((u32)(id >> 32) * 0x85EBCA77u)) & ~1u;
        nonce = (v2u){a, b | (~a & 1u)};
    }
    const u32 flag_w = t < 8 ? WS_FLAG + (u32)(t * 256 + rbk) * 16 : 0x7FFFFFF0u;
    FENCE();
    if (m < 30) *(LAS v4u*)(L + L_U + c1 * U_S + 16 * m) = (v4u){0u, 0u, 0u, 0u};
    if (m < 8) *(LAS v4u*)(L + L_KC + (c1 * 4 + (m >> 1)) * KC_S + (m & 1) * 16) = (v4u){0u, 0u, 0u, 0u};

    LAS const unsigned char* abase = L + L_KC + (c1 * 4 + (n16 >> 2)) * KC_S + 56 - 16 * g + 8 * (n16 & 3) + 128 * rq;
    LAS const unsigned char* bbase = L + L_U + c1 * U_S + 384 + 32 * n16 + 16 * g - 128 * rq;
    LAS unsigned char* pbase = L + L_P + (rq * 2 + c1) * 4096 + (g * 16 + ((n16 + 4 * g) & 15)) * 16;
    const int eg = l & 3, en = l >> 2;
    LAS const unsigned char* prd = L + L_P + rq * 1024 + (eg * 16 + ((en + 4 * eg) & 15)) * 16;
    LAS const unsigned char* urd = L + L_U + 480 + w * 512 + l * 8;
    LAS unsigned char* ust = L + L_U + (t & 1) * U_S + 480 + (t >> 1) * 16;
    LAS unsigned char* kcw = L + L_KC + c1 * 4 * KC_S + 32 + 16 * m;
    LAS u32* ti = (LAS u32*)(L + L_TI) + (4 * (t & 1)) * 260 + (t >> 1);
    LAS const unsigned char* tr = L + L_TO + ((l & 15) >> 2) * 1088 + (w * 64 + (l >> 4) * 16 + (l & 3) * 4) * 2;
    const u32 pub_col = (u32)((w * 16 + en) * 16384 + wg * 64 + eg * 16);
    const u32 get_p = (u32)((t >> 1) * 64 + (t & 1) * 16);
    const unsigned char* gu = ws + (wg >> 1) * 16384 + (wg & 1) * 32;
    const unsigned char* gy = ws + (rbk >> 1) * 16384 + (rbk & 1) * 32;
    const unsigned char* flag_b = ws + WS_FLAG + (wg >> 5) * 4096;
    const u32 flag_o = (u32)(t >> 1) * 16;
    const u32 hint_w = m < 8 ? WS_HINT + (u32)m * 1024 : 0x7FFFFFF0u;
    const unsigned char* hb = ws + WS_HINT + (wg >> 5) * 1024;
    const u32 ho = (u32)(w * 128 + (l & 7) * 16), ho0 = ho + (u32)(l & 8) * 64;
    const u32 hoy = w == 4 ? (u32)l * 16 : ho, hou = w == 4 ? (u32)(512 + (l & 31) * 16) : ho;

    {
        f2 w0 = {lo2f(sw.x), lo2f(sw.z)}, w1 = {hi2f(sw.x), hi2f(sw.z)}, w2 = {lo2f(sw.y), lo2f(sw.w)}, w3 = {hi2f(sw.y), hi2f(sw.w)};
        asm volatile("" : "+v"(w0), "+v"(w1), "+v"(w2), "+v"(w3));
        lds_bar();
        STAMP(1);
        LAS const unsigned char* kp = L + L_P + 4 * hh * KV_S + cp * 4;
        u32 kr[7], vr[4];
#pragma unroll
        for (int i = 0; i < 7; ++i) kr[i] = *(LAS const u32*)(kp + i * KV_S);
#pragma unroll
        for (int i = 0; i < 4; ++i) vr[i] = *(LAS const u32*)(kp + (11 + i) * KV_S);
        f2 kf[7], kv[4];
        const u32 km = rbk + hh ? ~0u : 0u;
        kr[0] &= km; kr[1] &= km; kr[2] &= km;
#pragma unroll
        for (int i = 0; i < 7; ++i) kf[i] = up2(kr[i]);
#pragma unroll
        for (int r = 0; r < 4; ++r) kv[r] = fma2(kf[r + 3], w0, fma2(kf[r + 2], w1, fma2(kf[r + 1], w2, kf[r] * w3))) * up2(vr[r]);
        const u32 p00 = pk_bf(kv[0].x, kv[1].x), p01 = pk_bf(kv[2].x, kv[3].x);
        const u32 p10 = pk_bf(kv[0].y, kv[1].y), p11 = pk_bf(kv[2].y, kv[3].y);
        const u32 s0 = hh ? p00 : p10, s1 = hh ? p01 : p11;
        const u32 r0 = dppu<0xB1>(s0), r1 = dppu<0xB1>(s1);
        v4u o = hh ? (v4u){r0, r1, p10, p11} : (v4u){p00, p01, r0, r1};
        asm volatile("" : "+v"(o));
            box_put(o, box, pub_row, 0);
        FENCE();
    }
    STAMP(2); STAMP(3);
    f2 C, e128, ea, eb;
    {
        const float dt = rb(exp_acc(bf2f(r_ldt)));
        const float are = rb(exp_acc(bf2f(r_lar)));
        const float zr2 = -(are * dt) * 1.44269504088896340736f;
        const float zi = bf2f(r_aim) * dt;
        const float chi = 0.15915494309189533577f, clo = 6.420638326565253e-09f;
        const float fh = zi * chi;
        const float fl2 = fmaf(zi, clo, fmaf(zi, chi, -fh));
        const u32 am = max(max(r_cre & 0x7FFFu, r_cim & 0x7FFFu), max(r_cre2 & 0x7FFFu, r_cim2 & 0x7FFFu));
        const float mx = bf2f(wave_maxu(am)) * dt;
        int ec = __builtin_amdgcn_frexp_expf(mx);
        ec = ec < -100 ? -100 : ec;
        const float sA = __builtin_amdgcn_ldexpf(1.0f, 13 - ec);
        C = (f2){bf2f(r_cre), -bf2f(r_cim)} * sp2(dt) * sp2(sA);
        e128 = cexp_p(zr2, -fh, -fl2, 128.f);
        ea = cexp_p(zr2, -fh, -fl2, c1 ? 1.f : 4.f);
        eb = cexp_p(zr2, -fh, -fl2, c1 ? 2.f : 512.f);
        if (l == 0) *(LAS int*)(L + L_KCMP + ((w >> 1) & 1) * KCMP_S + 2824) = ec;
        asm volatile("" : "+v"(C), "+v"(e128), "+v"(ea), "+v"(eb));
        SB();
    }
    STAMP(4);
    {
        if (m == 0) fprv = (v2u){0u, 0u};
        const u32 d[6] = {fprv.x, fprv.y, fcur.x, fcur.y, fcur.z, fcur.w};
        write_copies(kcw, d);
    }
    FENCE();
    auto body = [&](auto hc, auto wc) __attribute__((always_inline)) {
    constexpr bool HH = decltype(hc)::v, WC = decltype(wc)::v;
    asm volatile("" ::: "memory");
    v4u fl, q16;
    v4u af[2][8];
    v4u pc;
    {
        LAS unsigned char* tb = L + L_TB + ((w >> 1) & 1) * TB_S + pn * 4;
#define PUT(rowi, v) *(LAS u32*)(tb + (rowi) * TB_R) = pk_h((v).x, (v).y)
#define MIDDLE() do { \
        SB(); \
        vm_wait();                 \
        asm volatile("" :: "v"(pfd)); \
        lds_bar(); \
        if (!WC && w == 0) box_put((v4u){nonce.x, nonce.y, 0u, 0u}, box, flag_w, 0); \
        tfl = now(); \
        box_put(sent4, box, ini_u, 2 * MB2); \
        q16 = *(const v4u*)(qin + (size_t)row * DM + 8 * l); \
        FENCE(); \
        STAMP(5); \
        load_afrags(af, abase); } while (0)
#define MIDDLE2() do { \
        SB(); \
        wait_until(tfl, D_FL + (31 - (blockIdx.x >> 3)) * FL_K); \
        fl = box_req(flag_b, flag_o); \
        FENCE(); } while (0)
        u32 tfl;
        if constexpr (!WC) {
            f2 G = cmul(C, eb);
            f2 e8 = cmul(ea, ea); e8.y = -e8.y;
            f2 B = {16384.f, 0.f};
#pragma unroll
            for (int r = 0; r < 16; ++r) {
                const int h = r + 4;
                if (r == MID_A) { asm volatile("" : "+v"(G), "+v"(B), "+v"(e8)); MIDDLE(); }
                if (r == MID_B) { asm volatile("" : "+v"(G), "+v"(B), "+v"(e8)); MIDDLE2(); }
                PUT(41 + r, B); if (r < 15) B = cmul(B, e8);
                if (h < 8) { PUT(24 + 2 * (h - 4), G); const f2 G4 = cmul(G, ea); PUT(25 + 2 * (h - 4), G4); }
                else if (h <= 16) PUT(24 + h, G);
                if (h < 16) G = cmul(G, e128);
            }
        } else {
            f2 e[8];
            e[1] = ea; e[2] = eb; e[3] = cmul(e[2], e[1]); e[4] = cmul(e[2], e[2]);
            e[5] = cmul(e[4], e[1]); e[6] = cmul(e[4], e[2]); e[7] = cmul(e[4], e[3]);
            f2 G = C;
#pragma unroll
            for (int h = 0; h < 4; ++h) {
                if (h == MID_C) { asm volatile("" : "+v"(G)); MIDDLE(); }
                if (h == MID_D) { asm volatile("" : "+v"(G)); MIDDLE2(); }
                if (h < 2) {
                    PUT(8 * h, G);
#pragma unroll
                    for (int a = 1; a < 8; ++a) { const f2 v = cmul(G, e[a]); PUT(8 * h + a, v); }
                } else {
                    PUT(16 + 4 * (h - 2), G);
#pragma unroll
                    for (int a = 1; a < 4; ++a) { const f2 v = cmul(G, e[2 * a]); PUT(16 + 4 * (h - 2) + a, v); }
                }
                if (h < 3) G = cmul(G, e128);
            }
        }
#undef PUT
    }
    STAMP(6);

    f4 acc[8];
    v4u bq[2][4];
    v4u py, py2;
#define EPILOGUE(BOXI, S0, S1, E) do { \
        f4 pp[8]; \
        _Pragma("unroll") for (int i = 0; i < 8; ++i) pp[i] = *(LAS const f4*)(prd + i * 4096); \
        const f4 y0 = __builtin_elementwise_fma((pp[0] + pp[2]) + (pp[4] + pp[6]), sp4(S0), e0); \
        const f4 y1 = __builtin_elementwise_fma((pp[1] + pp[3]) + (pp[5] + pp[7]), sp4(S1), e1); \
        v4u o; \
        _Pragma("unroll") for (int r = 0; r < 4; ++r) o[r] = clampS(pk_bf_s(y0[r], y1[r])); \
        box_put(o, box, pub_col, (BOXI) * MB2); \
        XS(1, E); \
        FENCE(); \
        __builtin_amdgcn_s_setprio(0); } while (0)
#define ZERO_ACC() do { _Pragma("unroll") for (int i = 0; i < 8; ++i) acc[i] = (f4){0.f, 0.f, 0.f, 0.f}; } while (0)
#define PUT_TI(p, o) do { ti[(o)] = p.x; ti[260 + (o)] = p.y; ti[520 + (o)] = p.z; ti[780 + (o)] = p.w; } while (0)
#define KGEN_MM(ch, mt, bfr) do { \
        LAS const unsigned char* ta = L + L_TB + (ch) * TB_S + (16 * (mt) + n16) * TB_R + 16 * g; \
        v4u afr[8]; \
        _Pragma("unroll") for (int s = 0; s < 8; ++s) afr[s] = *(LAS const v4u*)(ta + 64 * s); \
        f4 d0 = {0.f, 0.f, 0.f, 0.f}, d1 = {0.f, 0.f, 0.f, 0.f}; \
        _Pragma("unroll") for (int s = 0; s < 8; s += 2) { d0 = mm<false>(afr[s], bfr[s], d0); d1 = mm<false>(afr[s + 1], bfr[s + 1], d1); } \
        const f4 dacc = d0 + d1; \
        const int pb = 8 * n16 + ((mt) == 0 ? 4 * (g & 1) + 128 * (g >> 1) : (mt) == 1 ? (g < 2 ? 256 + 128 * g : 256 * g) : 1024 + 512 * g); \
        const int s1 = (mt) == 0 ? 1 : (mt) == 1 ? (g < 2 ? 2 : 4) : 128, s2 = (mt) == 0 ? 2 : (mt) == 1 ? (g < 2 ? 4 : 128) : 256; \
        _Pragma("unroll") for (int i = 0; i < 4; ++i) { \
            int p = pb + (i & 1) * s1 + (i >> 1) * s2; \
            p = p > 2048 ? 2064 : p; \
            u32 ad = __builtin_bit_cast(u32, (float)p) >> 15; \
            if (i == 0) ad = ad > 0x7EFEu ? ad : 0x7EFEu; \
            *(LAS _Float16*)(L + L_KCMP + (ch) * KCMP_S - 0x7EFE + ad) = (_Float16)(dacc[i] * 1.9073486328125e-06f);     \
        } } while (0)
    u32 xd[6];
#define EXPAND1() do { \
        u32* d = xd; \
        const float f0 = (float)(8 * m - 4); \
        const u32 kb = (u32)(L_KCMP + c1 * KCMP_S - 2 * 0x3F7F); \
        _Pragma("unroll") for (int i = 0; i < 6; ++i) { \
            float fa = f0 + (float)(2 * i), fb = f0 + (float)(2 * i + 1); \
            if (i < 3) fa = fmaxf(fa, 0.99609375f); \
            if (i < 2) fb = fmaxf(fb, 0.99609375f); \
            const u32 cd = pk_bf(fa, fb); \
            u32 a0, a1; \
            asm("v_mad_u32_u16 %0, %2, 2, %3\n\tv_mad_u32_u16 %1, %2, 2, %3 op_sel:[1,0,0,0]" : "=&v"(a0), "=v"(a1) : "v"(cd), "s"(kb)); \
            us2 v; v.x = *(LAS const u16*)(L + a0); v.y = *(LAS const u16*)(L + a1); \
            d[i] = __builtin_bit_cast(u32, v); \
        } \
        if (m == 0) { d[0] = 0u; d[1] = 0u; } \
        asm volatile("" : "+v"(d[0]), "+v"(d[1]), "+v"(d[2]), "+v"(d[3]), "+v"(d[4]), "+v"(d[5])); \
        SB(); } while (0)
#define EXPAND2() do { SB(); write_copies(kcw, xd); lds_bar(); SB(); load_afrags(af, abase); SB(); } while (0)
#define TK(c) ((tpc * (u32)(c)) >> 16)
#define HPUT(e, id, val) do { const u32 hv_ = (val); XV(0, e, hv_); __builtin_amdgcn_raw_buffer_store_b32(hv_, box, hint_w, (e) * 8192 + (id) * 4, STA); FENCE(); } while (0)
#define HB(e) (hb + (e) * 8192)
#define DUE(T, n) (~((n) - (T)) >> 31)
    {
        asm volatile("s_waitcnt vmcnt(0)" : "+v"(fl) :: "memory");
#pragma unroll 1
        for (u32 sp = 0; sp < (1u << 16); ++sp) {
            const unsigned long long bad = __builtin_amdgcn_ballot_w64(fl.x != nonce.x || fl.y != nonce.y);
            if (!bad) break;
            unsigned long long sv;
            asm volatile("s_sleep 1\n\ts_mov_b64 %1, exec\n\ts_mov_b64 exec, %4\n\tglobal_load_dwordx4 %0, %2, %3 " LDP "\n\ts_mov_b64 exec, %1\n\ts_waitcnt vmcnt(0)"
                         : "+v"(fl), "=&s"(sv) : "v"(flag_o), "s"(flag_b), "s"(bad) : "memory");
        }
        asm volatile("" : "+v"(q16));
        pc = box_req(gu, get_p);
        FENCE();
        STAMP(7);
    }
    lds_bar();
#define KGEN2() do { if (w < 6) { \
        const int ch = w >= 3; \
        LAS const unsigned char* tbb = L + L_TB + ch * TB_S + (41 + n16) * TB_R + 16 * g; \
        v4u bfr[8]; \
        _Pragma("unroll") for (int s = 0; s < 8; ++s) bfr[s] = *(LAS const v4u*)(tbb + 64 * s); \
        const int mt = w - 3 * ch; \
        KGEN_MM(ch, mt, bfr); \
    } SB(); } while (0)
    KGEN2();
    u32 tpc;
    {
        const u32 c_n = now(), r_n = rtime();
        const float q = (float)(r_n - r_s) * 65536.f * __builtin_amdgcn_rcpf((float)(c_n - c_s));
        tpc = __builtin_amdgcn_readfirstlane((u32)fminf(fmaxf(q, 2200.f), 4400.f));
    }
    SB();
    f2 x[4] = {up2(q16.x), up2(q16.y), up2(q16.z), up2(q16.w)};
    f2 gate[4];
    v4u hy, hu;
    u32 tq, rq0, nw;
#define LN_Q() do { \
            float rs0, nb0; \
            ln_stats(x, 1.0f, rs0, nb0); \
            _Pragma("unroll") for (int i = 0; i < 4; ++i) gate[i] = fma2(x[i], sp2(rs0), sp2(nb0)); \
            asm volatile("" : "+v"(gate[0]), "+v"(gate[1]), "+v"(gate[2]), "+v"(gate[3])); \
            SB(); } while (0)
#define LNQ_WC LN_Q()
#define Y0_EARLY(BOXI) do { \
            tq = hint_chk((BOXI) == 1 ? 2 : 0, hy, rq0); \
            nw = DUE(tq, rq0); \
            py = sent4; py2 = sent4; \
            box_req_if(py, gy + (BOXI) * MB2, get_p, nw); box_req_if(py2, gy + (BOXI) * MB2 + 8192, get_p, nw); \
            SB(); } while (0)
    {
        STAMP(8);
        asm volatile("s_waitcnt vmcnt(0)" : "+v"(pc) :: "memory");
        *(LAS v4u*)ust = pc;
        box_put(sent4, box, ini_y, 3 * MB2);
        lds_bar();
        STAMP(9);
        if constexpr (WC) HPUT(0, wg, rtime() + TK(C_E0A) + T_VIS);
        load_b<0>(bq, bbase);
        const v2u i0 = *(LAS const v2u*)(urd), i1 = *(LAS const v2u*)(urd + U_S);
        ZERO_ACC();
        SB();
        conv_diag<true, 0, 0>(af, bq, acc, pbase);
        conv_diag<true, 0, 1>(af, bq, acc, pbase);
        f4 e0 = (f4){lo2f(i0.x), hi2f(i0.x), lo2f(i0.y), hi2f(i0.y)} * sp4(lo2f(dA2));
        f4 e1 = (f4){lo2f(i1.x), hi2f(i1.x), lo2f(i1.y), hi2f(i1.y)} * sp4(hi2f(dA2));
        asm volatile("" : "+v"(e0), "+v"(e1));
        SB();
        put_tile(acc[3], pbase, 3);
        if constexpr (!WC) {
            __builtin_amdgcn_s_setprio(3);
            lds_bar();
            EPILOGUE(1, 1.0f, 1.0f, 0);
            if constexpr (!HH) { hy = hint_req(HB(0), ho0); FENCE(); }
        } else {
            lds_bar();
            HPUT(1, wg, rtime() + TK(C_A1) + T_VIS);
            LNQ_WC;
        }
        STAMP(10);
        v4u b2[2][4];
        conv_off<true, 0>(af, bq, acc);
        box_put(sent4, box, ini_u, 4 * MB2);
        FENCE();
        load_b<4>(b2, bbase);
        conv_off<true, 1>(af, bq, acc);
        box_put(sent4, box, ini_y, 5 * MB2);
        FENCE();
        lds_bar();
        STAMP(11);
        if constexpr (WC && !HH) HPUT(2, rbk, rtime() + TK(C_UA) + T_VIS);
#define Y0_TRY(BOXI) do { \
            u32 c = 0; \
            if (!nw) { rq0 = rtime(); c = DUE(tq, rq0); } \
            box_req_if(py, gy + (BOXI) * MB2, get_p, c); box_req_if(py2, gy + (BOXI) * MB2 + 8192, get_p, c); \
            nw |= c; SB(); } while (0)
#define Y0_LATE(E, BOXI) do { \
            if (!nw) hint_wait(tq, rq0, HB(E), ho0 RM(1u << (16 + (E)))); \
            box_req_if(py, gy + (BOXI) * MB2, get_p, nw ^ 1); box_req_if(py2, gy + (BOXI) * MB2 + 8192, get_p, nw ^ 1); \
            XV(2, E, nw ? rq0 : rtime()); \
            SB(); } while (0)
        conv_diag<true, 4, 0>(af, b2, acc, pbase);
        if constexpr (!HH && !WC) Y0_EARLY(1);
        conv_diag<true, 4, 1>(af, b2, acc, pbase);
        if constexpr (HH) { hy = hint_req(HB(1), hoy); FENCE(); }
        put_tile(acc[7], pbase, 7);
        if constexpr (WC) __builtin_amdgcn_s_setprio(3);
        lds_bar();
        if constexpr (WC) EPILOGUE(1, 1.0f, 1.0f, 1);
        else { if constexpr (!HH) Y0_LATE(0, 1); LN_Q(); }
        STAMP(12);
    }
    const float sc0 = __builtin_amdgcn_ldexpf(1.0f / USCALE, *(LAS const int*)(L + L_KCMP + 2824) - 7);
    const float sc1 = __builtin_amdgcn_ldexpf(1.0f / USCALE, *(LAS const int*)(L + L_KCMP + KCMP_S + 2824) - 7);

    v4u yAp;
#pragma unroll
    for (int it = 0; it < 3; ++it) {
        const int CR = it ? C_R1 : C_R0;
        const unsigned char* gub = gu + (2 + 2 * it) * MB2;
        {
            if (it == 1) {
                gate[0] = up2(yAp.x); gate[1] = up2(yAp.y); gate[2] = up2(yAp.z); gate[3] = up2(yAp.w);
                asm volatile("" : "+v"(gate[0]), "+v"(gate[1]), "+v"(gate[2]), "+v"(gate[3]));
            }
            if constexpr (HH) {
                if (it == 0) EXPAND1();
                tq = hint_chk(WC, hy, nw);
                hint_wait(tq, nw, HB(1 + 4 * it), hoy RM(1u << (17 + 4 * it)));
                py = box_req(gy + (1 + 2 * it) * MB2, get_p);
                XS(2, 1 + 4 * it);
                if (it < 2) {
                    if constexpr (WC) HPUT(3 + 4 * it, rbk, rtime() + T_RD + TK(CR) + T_VIS);
                    else { hu = hint_req(HB(2 + 4 * it), ho); pc = sent4; }
                }
                if (it == 0) EXPAND2();
                py = box_fin(it < 2, py, gy + (1 + 2 * it) * MB2, get_p, 1 + 4 * it);
                XS(3, 1 + 4 * it);
                PUT_TI(py, 0);
            } else if constexpr (!WC) {
                py = box_fin(1, py, gy + (1 + 2 * it) * MB2, get_p, 4 * it);
                PUT_TI(py, 0);
                py2 = box_fin(0, py2, gy + (1 + 2 * it) * MB2 + 8192, get_p, 4 * it);
                XS(3, 4 * it);
                PUT_TI(py2, 128);
            } else if (it == 0) {
                asm volatile("s_waitcnt vmcnt(5)" ::: "memory");
            }
            lds_bar();
            STAMP(13 + 7 * it);
            const v4u yv = *(LAS const v4u*)(L + L_TI + w * 1040 + l * 16);
            SB();
            if (it < 2) {
                if constexpr (HH && !WC) {
                    tq = hint_chk(0, hu, rq0);
                    nw = DUE(tq, rq0);
                    box_req_if(pc, gub, get_p, nw);
                    SB();
                }
            }
#define U0_TRY() do { \
                    u32 c = 0; \
                    if (!nw) { rq0 = rtime(); c = DUE(tq, rq0); } \
                    box_req_if(pc, gub, get_p, c); \
                    nw |= c; SB(); } while (0)
            const f2 yc[4] = {up2(yv.x), up2(yv.y), up2(yv.z), up2(yv.w)};
            float rs, nb;
            if (it == 0) {
#pragma unroll
                for (int i = 0; i < 4; ++i) x[i] = fma2(gate[i], yc[i], x[i]);
                yAp = yv;
            } else {
#pragma unroll
                for (int i = 0; i < 4; ++i) x[i] += yc[i];
            }
            if (it == 2) {
                const __amdgpu_buffer_rsrc_t ob = __builtin_amdgcn_make_buffer_rsrc(out, 0, SEQ * DM * 2, 0x00020000);
                v4u o = {pk_bf(x[0].x, x[0].y), pk_bf(x[1].x, x[1].y), pk_bf(x[2].x, x[2].y), pk_bf(x[3].x, x[3].y)};
                __builtin_amdgcn_raw_buffer_store_b128(o, ob, (u32)(row * DM * 2 + 16 * l), 0, 17);
                STAMP(14 + 7 * it);
                break;
            }
            if (it == 1) {
                ln_stats(x, 1.0f, rs, nb);
                if constexpr (HH && !WC) { SB(); U0_TRY(); }
#pragma unroll
                for (int i = 0; i < 4; ++i) x[i] = fma2(fma2(x[i], sp2(rs), sp2(nb)), gate[i], x[i]);
            }
            ln_stats(x, USCALE, rs, nb);
            v4u uo;
#pragma unroll
            for (int i = 0; i < 4; ++i) { const f2 u = fma2(x[i], sp2(rs), sp2(nb)); uo[i] = pk_h(u.x, u.y); }
            *(LAS v4u*)(L + L_TO + w * 1088 + l * 16) = uo;
            if constexpr (!WC) {
                SB();
                if constexpr (HH) U0_TRY();
                else hu = hint_req(HB(2 + 4 * it), ho);
                SB();
            } else if constexpr (HH) { SB(); hu = hint_req(HB(3 + 4 * it), hou); SB(); }
            lds_bar();
            const v2u lo2 = tr_read(tr), hi2 = tr_read(tr + 4 * 1088);
            box_put((v4u){clampS(lo2.x), clampS(lo2.y), clampS(hi2.x), clampS(hi2.y)}, box, pub_row, (2 + 2 * it) * MB2);
            FENCE();
            XS(1, 2 + 4 * it + HH);
            STAMP(14 + 7 * it);
        }
        {
            if constexpr (HH) {
                if constexpr (!WC) {
                    if (!nw) hint_wait(tq, rq0, HB(2 + 4 * it), ho RM(1u << (18 + 4 * it)));
                    box_req_if(pc, gub, get_p, nw ^ 1);
                    XV(2, 2 + 4 * it, nw ? rq0 : rtime());
                    if (!nw) vm_wait();
                    pc = box_fin(1, pc, gub, get_p, 2 + 4 * it);
                    XS(3, 2 + 4 * it);
                    *(LAS v4u*)ust = pc;
                }
            } else {
                if constexpr (WC) rq0 = rtime();
                if (it == 0) EXPAND1();
                if constexpr (!WC) {
                    tq = hint_chk(1, hu, nw);
                    hint_wait(tq, nw, HB(2 + 4 * it), ho RM(1u << (18 + 4 * it)));
                    pc = box_req(gub, get_p);
                    XS(2, 2 + 4 * it);
                }
                if (it == 0) EXPAND2();
                if constexpr (!WC) { pc = box_fin(0, pc, gub, get_p, 2 + 4 * it); XS(3, 2 + 4 * it); *(LAS v4u*)ust = pc; }
                else { wait_rt(rq0 + T_H1); tq = hint_chk(0, hint_req(HB(3 + 4 * it), hou), nw); }
            }
            lds_bar();
            STAMP(15 + 7 * it);
            u32 ts;
            if constexpr (WC) {
                ts = rtime();
                HPUT(4 + 4 * it, wg, ts + TK(C_E0) + T_VIS);
                pc = sent4;
                if constexpr (!HH) { rq0 = ts; nw = DUE(tq, ts); box_req_if(pc, gub, get_p, nw); SB(); }
            }
            load_b<0>(bq, bbase);
            const float d0 = lo2f(dB2) * (1.0f / USCALE), d1 = hi2f(dB2) * (1.0f / USCALE);
#define SKIP_LOAD() const v2u i0 = *(LAS const v2u*)(urd), i1 = *(LAS const v2u*)(urd + U_S)
#define SKIP_TERMS() \
            const h2 h00 = as_h2(i0.x), h01 = as_h2(i0.y), h10 = as_h2(i1.x), h11 = as_h2(i1.y); \
            f4 e0 = (f4){(float)h00.x, (float)h00.y, (float)h01.x, (float)h01.y} * sp4(d0); \
            f4 e1 = (f4){(float)h10.x, (float)h10.y, (float)h11.x, (float)h11.y} * sp4(d1); \
            asm volatile("" : "+v"(e0), "+v"(e1)); SB()
            ZERO_ACC();
            if constexpr (!WC) {
                SKIP_LOAD();
                SB();
                conv_diag<false, 0, 0>(af, bq, acc, pbase);
                conv_diag<false, 0, 1>(af, bq, acc, pbase);
                SKIP_TERMS();
                put_tile(acc[3], pbase, 3);
                __builtin_amdgcn_s_setprio(3);
                lds_bar();
                STAMP(16 + 7 * it);
                EPILOGUE(3 + 2 * it, sc0, sc1, 4 + 4 * it);
            } else {
                SB();
                conv_diag<false, 0, 0>(af, bq, acc, pbase);
                if constexpr (HH) { tq = hint_chk(2, hu, rq0); nw = DUE(tq, rq0); box_req_if(pc, gub, get_p, nw); SB(); }
                else U0_TRY();
                conv_diag<false, 0, 1>(af, bq, acc, pbase);
                put_tile(acc[3], pbase, 3);
                lds_bar();
                STAMP(16 + 7 * it);
                if (!nw) { hint_wait(tq, rq0, HB(3 + 4 * it), hou RM(1u << (19 + 4 * it))); rq0 = rtime(); }
                box_req_if(pc, gub, get_p, nw ^ 1);
                XV(2, 3 + 4 * it, rq0);
                ts += TK(C_01);
                HPUT(5 + 4 * it, wg, ts + max(0, (int)(rq0 + T_RD - ts)) + TK(C_E1) + T_VIS);
            }
            if (it == 0 && !WC) {
                if (w == 0) __builtin_amdgcn_raw_buffer_store_b64(~nonce, box, flag_w, 0, 16);
                FENCE();
            }
            if constexpr (!HH && !WC) { hy = hint_req(HB(4 + 4 * it), ho0); FENCE(); }
            conv_off<false, 0>(af, bq, acc);
            if constexpr (WC) { __builtin_amdgcn_s_sleep(E0_SL); SB(); }
            conv_off<false, 1>(af, bq, acc);
            if constexpr (WC) {
                pc = box_fin(1, pc, gub, get_p, 3 + 4 * it);
                XS(3, 3 + 4 * it);
                *(LAS v4u*)ust = pc;
            }
            STAMP(17 + 7 * it);
            lds_bar();
            STAMP(18 + 7 * it);
            if (WC && !HH && it == 0) HPUT(6, rbk, rtime() + TK(C_UB) + T_VIS);
            load_b<4>(bq, bbase);
            if constexpr (WC) {
                SKIP_LOAD();
                SB();
                conv_diag<false, 4, 0>(af, bq, acc, pbase);
                conv_diag<false, 4, 1>(af, bq, acc, pbase);
                SKIP_TERMS();
                if constexpr (HH) { hy = hint_req(HB(5 + 4 * it), hoy); FENCE(); }
                put_tile(acc[7], pbase, 7);
                __builtin_amdgcn_s_setprio(3);
                lds_bar();
                EPILOGUE(3 + 2 * it, sc0, sc1, 5 + 4 * it);
            } else {
                conv_diag<false, 4, 0>(af, bq, acc, pbase);
                if constexpr (!HH) Y0_EARLY(3 + 2 * it);
                conv_diag<false, 4, 1>(af, bq, acc, pbase);
                if constexpr (HH) { hy = hint_req(HB(5 + 4 * it), hoy); FENCE(); }
                put_tile(acc[7], pbase, 7);
                lds_bar();
                if constexpr (!HH) Y0_LATE(4 + 4 * it, 3 + 2 * it);
            }
            STAMP(19 + 7 * it);
        }
    }
    };
    if (H) { if (w < 4) body(BC<true>{}, BC<false>{}); else body(BC<true>{}, BC<true>{}); }
    else   { if (w < 4) body(BC<false>{}, BC<false>{}); else body(BC<false>{}, BC<true>{}); }
}

extern "C" void kernel_launch(void* const* d_in, const int* in_sizes, int n_in, void* d_out, int out_size, void* d_ws, size_t ws_size,
                              hipStream_t stream) {
    (void)hipFuncSetAttribute((const void*)k1_main, hipFuncAttributeMaxDynamicSharedMemorySize, L_END);
    k1_main<<<256, 512, L_END, stream>>>((const u16*)d_in[0], (const u16*)d_in[1], (const u16*)d_in[2], (const u16*)d_in[3], (const u16*)d_in[4],
                                         (const u16*)d_in[5], (const u16*)d_in[7], (const u16*)d_in[8], (const u16*)d_in[9], (const u16*)d_in[10],
                                         (const u16*)d_in[11], (const u16*)d_in[12], (u16*)d_out, (unsigned char*)d_ws);
}
```

```cpp
#include <hip/hip_runtime.h>

typedef unsigned short u16;
typedef unsigned u32;
typedef u32 v4u __attribute__((ext_vector_type(4)));
typedef u32 v2u __attribute__((ext_vector_type(2)));
typedef float f4 __attribute__((ext_vector_type(4)));
typedef float f2 __attribute__((ext_vector_type(2)));
typedef __bf16 bf8 __attribute__((ext_vector_type(8)));
typedef __bf16 bf2 __attribute__((ext_vector_type(2)));
typedef _Float16 h8 __attribute__((ext_vector_type(8)));
typedef _Float16 h2 __attribute__((ext_vector_type(2)));
typedef short s4 __attribute__((ext_vector_type(4)));
typedef unsigned short us2 __attribute__((ext_vector_type(2)));
#define LAS __attribute__((address_space(3)))
#define DI __device__ __forceinline__
extern "C" __device__ unsigned long long amd_dispatch_id() __asm("llvm.amdgcn.dispatch.id");

#define LDP "sc1"
#define STA 16
constexpr int SEQ = 2048, DM = 512, NS = 128;
constexpr u32 SENT = 0xFFFFFFFFu;
constexpr u32 MB2 = 2u * 1024u * 1024u;
constexpr u32 WS_FLAG = 6 * MB2;
constexpr float USCALE = 16.0f;
#define FLS 16
constexpr u32 WS_HINT = WS_FLAG + 2048 * FLS;
#define D_FL 1000
#define MID_A 0
#define MID_B 8
#define MID_C 0
#define MID_D 2
#define FL_W 400
#define FL_K 25
#define T_VIS 30
#define T_RD 48
#define T_H1 35
#define C_E0A 1650
#define C_E0 1850
#define C_A1 3300
#define C_01 2600
#define C_E1 1200
#define C_R0 1000
#define C_R1 1150
#define C_UA 3050
#define C_UB 3100
#define CODE_B 65536
#define PF_W 7
#define TPC_MIN 2900.f
constexpr u32 HW = 800;

constexpr int KC_S = 4160;
constexpr int L_KC = 0;
constexpr int U_S = 4672;
constexpr int L_U = L_KC + 2 * 4 * KC_S;
constexpr int L_TI = L_U + 2 * U_S;
constexpr int L_TO = L_TI + 8 * 1040;
constexpr int KCMP_S = 2832;
constexpr int L_KCMP = L_TO + 8 * 1088;
constexpr int TB_R = 544, TB_S = 57 * TB_R;
constexpr int L_TB = L_KCMP + 2 * KCMP_S;
constexpr int L_P = L_TB + 2 * TB_S;
constexpr int KV_S = 1056;
constexpr int L_END = L_P + 32768;

DI float bf2f(u32 h) { return __builtin_bit_cast(float, h << 16); }
DI float lo2f(u32 d) { return __builtin_bit_cast(float, d << 16); }
DI float hi2f(u32 d) { return __builtin_bit_cast(float, d & 0xFFFF0000u); }
DI f2 up2(u32 d) { return (f2){lo2f(d), hi2f(d)}; }
DI float rb(float x) { f2 t = {0.f, x}; return __builtin_bit_cast(float, __builtin_convertvector(t, bf2)); }
DI u32 pk_bf(float a, float b) { f2 t = {a, b}; return __builtin_bit_cast(u32, __builtin_convertvector(t, bf2)); }
DI u32 pk_h(float a, float b) { f2 t = {a, b}; return __builtin_bit_cast(u32, __builtin_convertvector(t, h2)); }
DI h2 as_h2(u32 d) { return __builtin_bit_cast(h2, d); }
DI u32 clampS(u32 d) { return d < 0xFFFFFFFEu ? d : 0xFFFFFFFEu; }
DI f2 fma2(f2 a, f2 b, f2 c) { return __builtin_elementwise_fma(a, b, c); }
DI f2 sp2(float a) { return (f2){a, a}; }
template <int CTRL, int RM = 0xf> DI float dppf(float v) {
    return __builtin_bit_cast(float, __builtin_amdgcn_update_dpp(0, __builtin_bit_cast(int, v), CTRL, RM, 0xf, true));
}
DI float rdl(float v, int l) { return __builtin_bit_cast(float, __builtin_amdgcn_readlane(__builtin_bit_cast(int, v), l)); }
DI void wave_sum2(float& a, float& b) {
    const auto sw = __builtin_amdgcn_permlane32_swap(__builtin_bit_cast(u32, a), __builtin_bit_cast(u32, b), false, false);
    float r = __builtin_bit_cast(float, (u32)sw[0]) + __builtin_bit_cast(float, (u32)sw[1]);
    r += dppf<0xB1>(r); r += dppf<0x4E>(r); r += dppf<0x141>(r); r += dppf<0x140>(r);
    asm volatile("s_nop 1\n\tv_add_f32_dpp %0, %0, %0 row_bcast:15 row_mask:0xa bank_mask:0xf\n\ts_nop 0" : "+v"(r));
    a = rdl(r, 31); b = rdl(r, 63);
}
template <int CTRL, int RM = 0xf> DI u32 dppu(u32 v) { return (u32)__builtin_amdgcn_update_dpp(0, (int)v, CTRL, RM, 0xf, true); }
DI u32 wave_maxu(u32 v) {
    v = max(v, dppu<0xB1>(v)); v = max(v, dppu<0x4E>(v)); v = max(v, dppu<0x141>(v)); v = max(v, dppu<0x140>(v));
    return max(max((u32)__builtin_amdgcn_readlane(v, 0), (u32)__builtin_amdgcn_readlane(v, 16)),
               max((u32)__builtin_amdgcn_readlane(v, 32), (u32)__builtin_amdgcn_readlane(v, 48)));
}
DI void ln_stats(const f2* d, float c, float scale, float& rs, float& nb) {
    const f2 s2 = (d[0] + d[1]) + (d[2] + d[3]);
    const f2 q2 = fma2(d[1], d[1], d[0] * d[0]) + fma2(d[3], d[3], d[2] * d[2]);
    float s = s2.x + s2.y, q = q2.x + q2.y;
    wave_sum2(s, q);
    const float md = s * (1.0f / DM);
    const float var = fmaxf(fmaf(-md, md, q * (1.0f / DM)), 0.f);
    const float mb = rb(c + md);
    const float sd = rb(__builtin_amdgcn_sqrtf(rb(rb(var) + 1e-5f)));
    rs = __builtin_amdgcn_rcpf(sd) * scale;
    nb = (c - mb) * rs;
}

DI bool has_sent(v4u v) { const u32 a = v.x > v.y ? v.x : v.y, b = v.z > v.w ? v.z : v.w; return (a > b ? a : b) == SENT; }
#define SB() __builtin_amdgcn_sched_barrier(0)
#define FENCE() do { asm volatile("" ::: "memory"); SB(); } while (0)
DI u32 now() { unsigned long long t_; asm volatile("s_memtime %0\n\ts_waitcnt lgkmcnt(0)" : "=s"(t_) :: "memory"); return (u32)t_; }
DI void wait_until(u32 t0, u32 d) {
#pragma unroll 1
    for (int i = 0; i < 512; ++i) if ((int)(now() - t0 - d) >= 0) break;
}
DI u32 rtime() { unsigned long long t_; asm volatile("s_memrealtime %0\n\ts_waitcnt lgkmcnt(0)" : "=s"(t_) :: "memory"); return (u32)t_; }
DI void wait_rt(u32 T) {
#pragma unroll 1
    for (int i = 0; i < 1024; ++i) if ((int)(rtime() - T) >= 0) break;
}
DI v4u box_req(const unsigned char* base, u32 off) {
    v4u v = {SENT, SENT, SENT, SENT};
    asm volatile("global_load_dwordx4 %0, %1, %2 " LDP : "+v"(v) : "v"(off), "s"(base) : "memory"); return v;
}
DI void box_req_if(v4u& v, const unsigned char* base, u32 off, u32 c) {
    asm volatile("s_cmp_eq_u32 %3, 0\n\ts_cbranch_scc1 .Lrq%=\n\tglobal_load_dwordx4 %0, %1, %2 " LDP "\n.Lrq%=:" : "+v"(v) : "v"(off), "s"(base), "s"(__builtin_amdgcn_readfirstlane(c)) : "memory", "scc");
}
DI v4u hint_req(const unsigned char* base, u32 off) {
    v4u v; asm volatile("global_load_dwordx4 %0, %1, %2 " LDP : "=v"(v) : "v"(off), "s"(base) : "memory"); return v;
}
DI u32 hint_chk(int N, v4u h, u32& n) {
    asm volatile("s_waitcnt vmcnt(%1)" : "+v"(h) : "i"(N) : "memory");
    n = rtime();
    const u32 b = n - HW;
    const u32 mx = wave_maxu(max(max(h.x - b, h.y - b), max(h.z - b, h.w - b)));
    return mx <= 2 * HW ? b + mx : n + 0x40000000u;
}
DI void hint_wait(u32 T, u32 n, const unsigned char* base, u32 off) {
    if (__builtin_expect((int)(T - n) > (int)(2 * HW), 0))
#pragma unroll 1
    for (int i = 0; i < 6 && (int)(T - n) > (int)(2 * HW); ++i) {
        T = hint_chk(0, hint_req(base, off), n);
    }
    if ((int)(T - n) <= (int)(2 * HW)) wait_rt(T);
}
DI v4u box_fin(int N, v4u v, const unsigned char* base, u32 off) {
    asm volatile("s_waitcnt vmcnt(%1)" : "+v"(v) : "i"(N) : "memory");
    unsigned long long bad = __builtin_amdgcn_ballot_w64(has_sent(v));
    if (__builtin_expect(bad != 0, 0))
#pragma unroll 1
    for (u32 sp = 0; sp < (1u << 16) && bad; ++sp, bad = __builtin_amdgcn_ballot_w64(has_sent(v))) {
        unsigned long long sv;
        asm volatile("s_sleep 2\n\ts_mov_b64 %1, exec\n\ts_mov_b64 exec, %4\n\tglobal_load_dwordx4 %0, %2, %3 " LDP "\n\ts_mov_b64 exec, %1\n\ts_waitcnt vmcnt(0)"
                     : "+v"(v), "=&s"(sv) : "v"(off), "s"(base), "s"(bad) : "memory");
    }
    return v;
}
DI void box_put(v4u v, __amdgpu_buffer_rsrc_t r, u32 off, u32 soff) {
    asm volatile("" : "+v"(v));
    __builtin_amdgcn_raw_buffer_store_b128(v, r, off, soff, STA);
    asm volatile("s_nop 1" :: "v"(v) : "memory");
}
DI void glds16(const void* base, u32 off, u32 lds) {
    asm volatile("s_mov_b32 m0, %2\n\ts_nop 0\n\tglobal_load_lds_dwordx4 %0, %1" :: "v"(off), "s"(base), "s"(lds) : "memory");
}
DI void lds_bar() { asm volatile("s_waitcnt lgkmcnt(0)\n\ts_barrier" ::: "memory"); }
DI void vm_wait() { asm volatile("s_waitcnt vmcnt(0)" ::: "memory"); }

DI void write_copies(LAS unsigned char* kc  , const u32* d) {
    u32 o[5];
#pragma unroll
    for (int i = 0; i < 5; ++i) o[i] = __builtin_amdgcn_alignbit(d[i + 1], d[i], 16);
    *(LAS v4u*)(kc) = (v4u){d[2], d[3], d[4], d[5]};
    *(LAS v4u*)(kc + KC_S) = (v4u){o[1], o[2], o[3], o[4]};
    *(LAS v4u*)(kc + 2 * KC_S) = (v4u){d[1], d[2], d[3], d[4]};
    *(LAS v4u*)(kc + 3 * KC_S) = (v4u){o[0], o[1], o[2], o[3]};
}

template <bool BF> DI f4 mm(v4u a, v4u b, f4 c) {
    if constexpr (BF) return __builtin_amdgcn_mfma_f32_16x16x32_bf16(__builtin_bit_cast(bf8, a), __builtin_bit_cast(bf8, b), c, 0, 0, 0);
    else return __builtin_amdgcn_mfma_f32_16x16x32_f16(__builtin_bit_cast(h8, a), __builtin_bit_cast(h8, b), c, 0, 0, 0);
}
DI v2u tr_read(LAS const unsigned char* p) { return __builtin_bit_cast(v2u, __builtin_amdgcn_ds_read_tr16_b64_v4i16((LAS s4*)p)); }

template <int P0 = 0, int P1 = 8> DI void load_afrags(v4u (&a)[2][8], LAS const unsigned char* abase) {
#pragma unroll
    for (int p = P0; p < P1; ++p)
#pragma unroll
        for (int ri = 0; ri < 2; ++ri) {
            const v2u lo = tr_read(abase + 512 * p + 64 * ri + 8), hi = tr_read(abase + 512 * p + 64 * ri);
            a[ri][p] = (v4u){lo.x, lo.y, hi.x, hi.y};
        }
}
template <int Q0> DI void load_b(v4u (&bq)[2][4], LAS const unsigned char* bbase) {
#pragma unroll
    for (int q = 0; q < 4; ++q) {
#pragma unroll
        for (int ri = 0; ri < 2; ++ri) { bq[ri][q] = *(LAS const v4u*)(bbase + 512 * (Q0 + q) + 64 * (1 - ri)); SB(); }
    }
}
template <bool BF, int Q0, int q, int TA, int TB> DI void conv_q(const v4u (&a)[2][8], const v4u (&bq)[2][4], f4 (&acc)[8]) {
#pragma unroll
    for (int ri = 0; ri < 2; ++ri)
#pragma unroll
        for (int T = TA; T < TB; ++T) acc[T] = mm<BF>(a[ri][T - Q0 - q], bq[ri][q], acc[T]);
}
DI void put_tile(f4 v, LAS unsigned char* pbase, int T) { *(LAS f4*)(pbase + (T & 3) * 1024) = v; }
template <bool BF, int Q0, int PART> DI void conv_diag(const v4u (&a)[2][8], const v4u (&bq)[2][4], f4 (&acc)[8], LAS unsigned char* pbase) {
    if constexpr (PART == 0) {
        conv_q<BF, Q0, 0, Q0, Q0 + 4>(a, bq, acc);
        SB();
        conv_q<BF, Q0, 1, Q0 + 1, Q0 + 4>(a, bq, acc);
        SB();
        put_tile(acc[Q0], pbase, Q0);
        asm volatile("" : "+v"(acc[Q0 + 1]), "+v"(acc[Q0 + 2]), "+v"(acc[Q0 + 3]));
        SB();
    } else {
        conv_q<BF, Q0, 2, Q0 + 2, Q0 + 4>(a, bq, acc);
        SB();
        put_tile(acc[Q0 + 1], pbase, Q0 + 1);
        acc[Q0 + 3] = mm<BF>(a[0][0], bq[0][3], acc[Q0 + 3]);
        SB();
        put_tile(acc[Q0 + 2], pbase, Q0 + 2);
        acc[Q0 + 3] = mm<BF>(a[1][0], bq[1][3], acc[Q0 + 3]);
        SB();
    }
}
template <bool BF, int PART> DI void conv_off(const v4u (&a)[2][8], const v4u (&bq)[2][4], f4 (&acc)[8]) {
    conv_q<BF, 0, 2 * PART, 4, 8>(a, bq, acc);
    conv_q<BF, 0, 2 * PART + 1, 4, 8>(a, bq, acc);
    asm volatile("" : "+v"(acc[4]), "+v"(acc[5]), "+v"(acc[6]), "+v"(acc[7]));
    SB();
}
DI u32 pk_bf_s(float a, float b) { u32 r; asm("v_cvt_pk_bf16_f32 %0, %1, %2" : "=v"(r) : "v"(a), "v"(b)); return r; }
DI f4 sp4(float a) { return (f4){a, a, a, a}; }

DI f2 cmul(f2 a, f2 b) {
    f2 r;
    asm("v_pk_mul_f32 %0, %1, %2 op_sel:[0,0] op_sel_hi:[0,1]\n\t"
        "v_pk_fma_f32 %0, %1, %2, %0 op_sel:[1,1,0] op_sel_hi:[1,0,1] neg_lo:[0,1,0]" : "=&v"(r) : "v"(a), "v"(b));
    return r;
}
DI f2 cexp_p(float zr2, float fh, float fl, float p) {
    const float mag = __builtin_amdgcn_exp2f(zr2 * p);
    const float an = fmaf(fl, p, __builtin_amdgcn_fractf(fh * p));
    return (f2){__builtin_amdgcn_cosf(an), __builtin_amdgcn_sinf(an)} * sp2(mag);
}
DI float exp_acc(float x) {
    const float L = 1.44269504088896340736f, Ll = 1.92596299112661746e-08f;
    const float h = x * L;
    const float lo = fmaf(x, Ll, fmaf(x, L, -h));
    const float e = __builtin_amdgcn_exp2f(h);
    return fmaf(e * lo, 0.69314718055994530942f, e);
}

template <bool V> struct BC { static constexpr bool v = V; };

__global__ void __launch_bounds__(512, 2)
k1_main(const u16* __restrict__ qin, const u16* __restrict__ kin, const u16* __restrict__ vin, const u16* __restrict__ shk,
        const u16* __restrict__ ssmk, const u16* __restrict__ ssmD, const u16* __restrict__ lar, const u16* __restrict__ aim,
        const u16* __restrict__ cre, const u16* __restrict__ cim, const u16* __restrict__ ldt, const u16* __restrict__ dsk,
        u16* __restrict__ out, unsigned char* ws) {
    extern __shared__ __attribute__((aligned(16))) unsigned char smem[];
    LAS unsigned char* L = (LAS unsigned char*)smem;
    const int t = threadIdx.x, l = t & 63;
    const int w = __builtin_amdgcn_readfirstlane(t >> 6);
    const int wg = (blockIdx.x & 7) * 32 + (blockIdx.x >> 3);
    const int H = (wg >> 1) & 1;
    const int rbk = H * 128 + (wg >> 2) * 2 + (wg & 1);
    const int c1 = w >> 2;
    const int m = t & 255;
    const int rq = w & 3;
    const int n16 = l & 15, g = l >> 4;
    const __amdgpu_buffer_rsrc_t box = __builtin_amdgcn_make_buffer_rsrc(ws, 0, (int)(WS_HINT + 81920), 0x00020000);
    u32 pfd;
    {
        unsigned long long pc;
        asm volatile("s_getpc_b64 %0" : "=s"(pc));
        const u32 po = min((u32)((l * 16 + ((blockIdx.x >> 3) & 15)) * 128), (u32)(CODE_B - 4));
        if (w == PF_W) asm volatile("global_load_dword %0, %1, %2" : "=v"(pfd) : "v"(po), "s"(pc) : "memory");
        else asm volatile("" : "=v"(pfd));
    }
    const v4u sent4 = {SENT, SENT, SENT, SENT};
    const u32 pub_row = (u32)((t >> 2) * 16384 + rbk * 64 + (t & 3) * 16);
    {
        const u32 vo = (u32)l * 16u;
        glds16(kin + (size_t)(8 * rbk + w) * DM, vo, (u32)(L_P + (3 + w) * KV_S));
        glds16(vin + (size_t)(8 * rbk + w) * DM, vo, (u32)(L_P + (11 + w) * KV_S));
        if (w < 3) {
            glds16(kin + (size_t)max(8 * rbk - 3 + w, 0) * DM, vo, (u32)(L_P + w * KV_S));
        }
    }
    const int cp = t >> 1, hh = t & 1;
    const v4u sw = *(const v4u*)(shk + cp * 8);
    FENCE();
    const int pch = 2 * wg + ((w >> 1) & 1), pn = t & 127;
    const u32 r_lar = lar[pch * NS + pn], r_aim = aim[pch * NS + pn], r_cre = cre[pch * NS + pn], r_cim = cim[pch * NS + pn];
    const u32 r_cre2 = cre[pch * NS + (pn ^ 64)], r_cim2 = cim[pch * NS + (pn ^ 64)];
    u32 vz; asm volatile("v_mov_b32 %0, 0" : "=v"(vz));
    const u32 r_ldt = ldt[pch + vz];
    const u32 dA2 = *(const u32*)(ssmD + 2 * wg + vz), dB2 = *(const u32*)(dsk + 2 * wg + vz);
    FENCE();
    const int chg = 2 * wg + c1;
    v4u fcur; v2u fprv;
    FENCE();
    const u32 ini_y = (u32)((rbk >> 1) * 16384 + w * 2048 + (rbk & 1) * 1024 + l * 16);
    const u32 ini_u = (u32)((wg >> 1) * 16384 + w * 2048 + (wg & 1) * 1024 + l * 16);
    FENCE();
    const u32 c_s = now(), r_s = rtime();
    const int row = 8 * rbk + w;
    v2u nonce;
    {
        const unsigned long long id = amd_dispatch_id(), dp = (unsigned long long)__builtin_amdgcn_dispatch_ptr();
        const u32 a = (u32)id * 0x9E3779B1u + (u32)(dp >> 32);
        const u32 b = ((u32)dp ^ ((u32)(id >> 32) * 0x85EBCA77u)) & ~1u;
        nonce = (v2u){a, b | (~a & 1u)};
    }
    const u32 flag_w = l == 0 ? WS_FLAG + (u32)(w * 256 + rbk) * FLS : 0x7FFFFFF0u;
    FENCE();
    if (m < 30) *(LAS v4u*)(L + L_U + c1 * U_S + 16 * m) = (v4u){0u, 0u, 0u, 0u};
    if (m < 8) *(LAS v4u*)(L + L_KC + (c1 * 4 + (m >> 1)) * KC_S + (m & 1) * 16) = (v4u){0u, 0u, 0u, 0u};

    LAS const unsigned char* abase = L + L_KC + (c1 * 4 + (n16 >> 2)) * KC_S + 56 - 16 * g + 8 * (n16 & 3) + 128 * rq;
    LAS const unsigned char* bbase = L + L_U + c1 * U_S + 384 + 32 * n16 + 16 * g - 128 * rq;
    LAS unsigned char* pbase = L + L_P + (rq * 2 + c1) * 4096 + (g * 16 + ((n16 + 4 * g) & 15)) * 16;
    const int eg = l & 3, en = l >> 2;
    LAS const unsigned char* prd = L + L_P + rq * 1024 + (eg * 16 + ((en + 4 * eg) & 15)) * 16;
    LAS const unsigned char* urd = L + L_U + 480 + w * 512 + l * 8;
    LAS unsigned char* ust = L + L_U + (t & 1) * U_S + 480 + (t >> 1) * 16;
    LAS unsigned char* kcw = L + L_KC + c1 * 4 * KC_S + 32 + 16 * m;
    LAS u32* ti = (LAS u32*)(L + L_TI) + (4 * (t & 1)) * 260 + (t >> 1);
    LAS const unsigned char* tr = L + L_TO + ((l & 15) >> 2) * 1088 + (w * 64 + (l >> 4) * 16 + (l & 3) * 4) * 2;
    const u32 pub_col = (u32)((w * 16 + en) * 16384 + wg * 64 + eg * 16);
    const u32 get_p = (u32)((t >> 1) * 64 + (t & 1) * 16);
    const unsigned char* gu = ws + (wg >> 1) * 16384 + (wg & 1) * 32;
    const unsigned char* gy = ws + (rbk >> 1) * 16384 + (rbk & 1) * 32;
    const unsigned char* flag_b = ws + WS_FLAG + (wg >> 5) * 256 * FLS;
    const u32 flag_o = (u32)(t >> 1) * FLS;
    const u32 hint_w = m < 8 ? WS_HINT + (u32)m * 1024 : 0x7FFFFFF0u;
    const unsigned char* hb = ws + WS_HINT + (wg >> 5) * 1024;
    const u32 ho = (u32)(w * 128 + (l & 7) * 16), ho0 = ho + (u32)(l & 8) * 64;
    const u32 hoy = w == 4 ? (u32)l * 16 : ho, hou = w == 4 ? (u32)(512 + (l & 31) * 16) : ho;

    {
        f2 w0 = {lo2f(sw.x), lo2f(sw.z)}, w1 = {hi2f(sw.x), hi2f(sw.z)}, w2 = {lo2f(sw.y), lo2f(sw.w)}, w3 = {hi2f(sw.y), hi2f(sw.w)};
        asm volatile("" : "+v"(w0), "+v"(w1), "+v"(w2), "+v"(w3));
        FENCE();
        fcur = *(const v4u*)(ssmk + (size_t)chg * SEQ + 8 * m);
        fprv = *(const v2u*)(ssmk + (size_t)chg * SEQ + (m > 0 ? 8 * m - 4 : 0));
        FENCE();
        box_put(sent4, box, ini_y, 1 * MB2);
        FENCE();
        lds_bar();
        LAS const unsigned char* kp = L + L_P + 4 * hh * KV_S + cp * 4;
        u32 kr[7], vr[4];
#pragma unroll
        for (int i = 0; i < 7; ++i) kr[i] = *(LAS const u32*)(kp + i * KV_S);
#pragma unroll
        for (int i = 0; i < 4; ++i) vr[i] = *(LAS const u32*)(kp + (11 + i) * KV_S);
        f2 kf[7], kv[4];
        const u32 km = rbk + hh ? ~0u : 0u;
        kr[0] &= km; kr[1] &= km; kr[2] &= km;
#pragma unroll
        for (int i = 0; i < 7; ++i) kf[i] = up2(kr[i]);
#pragma unroll
        for (int r = 0; r < 4; ++r) kv[r] = fma2(kf[r + 3], w0, fma2(kf[r + 2], w1, fma2(kf[r + 1], w2, kf[r] * w3))) * up2(vr[r]);
        const u32 p00 = pk_bf(kv[0].x, kv[1].x), p01 = pk_bf(kv[2].x, kv[3].x);
        const u32 p10 = pk_bf(kv[0].y, kv[1].y), p11 = pk_bf(kv[2].y, kv[3].y);
        const u32 s0 = hh ? p00 : p10, s1 = hh ? p01 : p11;
        const u32 r0 = dppu<0xB1>(s0), r1 = dppu<0xB1>(s1);
        v4u o = hh ? (v4u){r0, r1, p10, p11} : (v4u){p00, p01, r0, r1};
        asm volatile("" : "+v"(o));
        box_put(o, box, pub_row, 0);
        FENCE();
    }
    f2 C, e128, ea, eb;
    {
        const float dt = rb(exp_acc(bf2f(r_ldt)));
        const float are = rb(exp_acc(bf2f(r_lar)));
        const float zr2 = -(are * dt) * 1.44269504088896340736f;
        const float zi = bf2f(r_aim) * dt;
        const float chi = 0.15915494309189533577f, clo = 6.420638326565253e-09f;
        const float fh = zi * chi;
        const float fl2 = fmaf(zi, clo, fmaf(zi, chi, -fh));
        const u32 am = max(max(r_cre & 0x7FFFu, r_cim & 0x7FFFu), max(r_cre2 & 0x7FFFu, r_cim2 & 0x7FFFu));
        const float mx = bf2f(wave_maxu(am)) * dt;
        int ec = __builtin_amdgcn_frexp_expf(mx);
        ec = ec < -100 ? -100 : ec;
        const float sA = __builtin_amdgcn_ldexpf(1.0f, 13 - ec);
        C = (f2){bf2f(r_cre), -bf2f(r_cim)} * sp2(dt) * sp2(sA);
        e128 = cexp_p(zr2, -fh, -fl2, 128.f);
        ea = cexp_p(zr2, -fh, -fl2, c1 ? 1.f : 4.f);
        eb = c1 ? ea : cmul(e128, e128);
        eb = cmul(eb, eb);
        if (l == 0) *(LAS int*)(L + L_KCMP + ((w >> 1) & 1) * KCMP_S + 2824) = ec;
        asm volatile("" : "+v"(C), "+v"(e128), "+v"(ea), "+v"(eb));
        SB();
    }
    vm_wait();
    asm volatile("" :: "v"(pfd));
    __builtin_amdgcn_raw_buffer_store_b64(nonce, box, flag_w, 0, 16);
    const u32 tfl = now();
    box_put(sent4, box, ini_u, 2 * MB2);
    v4u q16 = *(const v4u*)(qin + (size_t)row * DM + 8 * l);
    FENCE();
    {
        if (m == 0) fprv = (v2u){0u, 0u};
        const u32 d[6] = {fprv.x, fprv.y, fcur.x, fcur.y, fcur.z, fcur.w};
        write_copies(kcw, d);
    }
    FENCE();
    auto body = [&](auto hc, auto wc) __attribute__((always_inline)) {
    constexpr bool HH = decltype(hc)::v, WC = decltype(wc)::v;
    asm volatile("" ::: "memory");
    v4u fl;
    v4u af[2][8];
    v4u pc;
    {
        LAS unsigned char* tb = L + L_TB + ((w >> 1) & 1) * TB_S + pn * 4;
#define PUT(rowi, v) *(LAS u32*)(tb + (rowi) * TB_R) = pk_h((v).x, (v).y)
#define MIDDLE() do { \
        SB(); \
        lds_bar(); \
        load_afrags(af, abase); } while (0)
#define MIDDLE2() do { \
        SB(); \
        wait_until(tfl, D_FL + ((wg >> 7) - c1) * FL_W + (31 - (blockIdx.x >> 3)) * FL_K); \
        fl = box_req(flag_b, flag_o); \
        FENCE(); } while (0)
        if constexpr (!WC) {
            f2 G = cmul(C, eb);
            f2 e8 = cmul(ea, ea); e8.y = -e8.y;
            f2 B = {16384.f, 0.f};
#pragma unroll
            for (int r = 0; r < 16; ++r) {
                const int h = r + 4;
                if (r == MID_A) { asm volatile("" : "+v"(G), "+v"(B), "+v"(e8)); MIDDLE(); }
                if (r == MID_B) { asm volatile("" : "+v"(G), "+v"(B), "+v"(e8)); MIDDLE2(); }
                PUT(41 + r, B); if (r < 15) B = cmul(B, e8);
                if (h < 8) { PUT(24 + 2 * (h - 4), G); const f2 G4 = cmul(G, ea); PUT(25 + 2 * (h - 4), G4); }
                else if (h <= 16) PUT(24 + h, G);
                if (h < 16) G = cmul(G, e128);
            }
        } else {
            f2 e[8];
            e[1] = ea; e[2] = eb; e[3] = cmul(e[2], e[1]); e[4] = cmul(e[2], e[2]);
            e[5] = cmul(e[4], e[1]); e[6] = cmul(e[4], e[2]); e[7] = cmul(e[4], e[3]);
            f2 G = C;
#pragma unroll
            for (int h = 0; h < 4; ++h) {
                if (h == MID_C) { asm volatile("" : "+v"(G)); MIDDLE(); }
                if (h == MID_D) { asm volatile("" : "+v"(G)); MIDDLE2(); }
                if (h < 2) {
                    PUT(8 * h, G);
#pragma unroll
                    for (int a = 1; a < 8; ++a) { const f2 v = cmul(G, e[a]); PUT(8 * h + a, v); }
                } else {
                    PUT(16 + 4 * (h - 2), G);
#pragma unroll
                    for (int a = 1; a < 4; ++a) { const f2 v = cmul(G, e[2 * a]); PUT(16 + 4 * (h - 2) + a, v); }
                }
                if (h < 3) G = cmul(G, e128);
            }
        }
#undef PUT
    }

    f4 acc[8];
    v4u bq[2][4];
    v4u py, py2;
#define EPILOGUE(BOXI, S0, S1, E) do { \
        f4 pp[8]; \
        _Pragma("unroll") for (int i = 0; i < 8; ++i) pp[i] = *(LAS const f4*)(prd + i * 4096); \
        const f4 y0 = __builtin_elementwise_fma((pp[0] + pp[2]) + (pp[4] + pp[6]), sp4(S0), e0); \
        const f4 y1 = __builtin_elementwise_fma((pp[1] + pp[3]) + (pp[5] + pp[7]), sp4(S1), e1); \
        v4u o; \
        _Pragma("unroll") for (int r = 0; r < 4; ++r) o[r] = clampS(pk_bf_s(y0[r], y1[r])); \
        box_put(o, box, pub_col, (BOXI) * MB2); \
        FENCE(); \
        __builtin_amdgcn_s_setprio(0); } while (0)
#define ZERO_ACC() do { _Pragma("unroll") for (int i = 0; i < 8; ++i) acc[i] = (f4){0.f, 0.f, 0.f, 0.f}; } while (0)
#define PUT_TI(p, o) do { ti[(o)] = p.x; ti[260 + (o)] = p.y; ti[520 + (o)] = p.z; ti[780 + (o)] = p.w; } while (0)
#define KGEN_MM(ch, mt, bfr) do { \
        LAS const unsigned char* ta = L + L_TB + (ch) * TB_S + (16 * (mt) + n16) * TB_R + 16 * g; \
        v4u afr[8]; \
        _Pragma("unroll") for (int s = 0; s < 8; ++s) afr[s] = *(LAS const v4u*)(ta + 64 * s); \
        f4 d0 = {0.f, 0.f, 0.f, 0.f}, d1 = {0.f, 0.f, 0.f, 0.f}; \
        _Pragma("unroll") for (int s = 0; s < 8; s += 2) { d0 = mm<false>(afr[s], bfr[s], d0); d1 = mm<false>(afr[s + 1], bfr[s + 1], d1); } \
        const f4 dacc = d0 + d1; \
        const int pb = 8 * n16 + ((mt) == 0 ? 4 * (g & 1) + 128 * (g >> 1) : (mt) == 1 ? (g < 2 ? 256 + 128 * g : 256 * g) : 1024 + 512 * g); \
        const int s1 = (mt) == 0 ? 1 : (mt) == 1 ? (g < 2 ? 2 : 4) : 128, s2 = (mt) == 0 ? 2 : (mt) == 1 ? (g < 2 ? 4 : 128) : 256; \
        _Pragma("unroll") for (int i = 0; i < 4; ++i) { \
            int p = pb + (i & 1) * s1 + (i >> 1) * s2; \
            p = p > 2048 ? 2064 : p; \
            u32 ad = __builtin_bit_cast(u32, (float)p) >> 15; \
            if (i == 0) ad = ad > 0x7EFEu ? ad : 0x7EFEu; \
            *(LAS _Float16*)(L + L_KCMP + (ch) * KCMP_S - 0x7EFE + ad) = (_Float16)(dacc[i] * 1.9073486328125e-06f);     \
        } } while (0)
    u32 xd[6];
#define EXPAND1() do { \
        u32* d = xd; \
        const float f0 = (float)(8 * m - 4); \
        const u32 kb = (u32)(L_KCMP + c1 * KCMP_S - 2 * 0x3F7F); \
        _Pragma("unroll") for (int i = 0; i < 6; ++i) { \
            float fa = f0 + (float)(2 * i), fb = f0 + (float)(2 * i + 1); \
            if (i < 3) fa = fmaxf(fa, 0.99609375f); \
            if (i < 2) fb = fmaxf(fb, 0.99609375f); \
            const u32 cd = pk_bf(fa, fb); \
            u32 a0, a1; \
            asm("v_mad_u32_u16 %0, %2, 2, %3\n\tv_mad_u32_u16 %1, %2, 2, %3 op_sel:[1,0,0,0]" : "=&v"(a0), "=v"(a1) : "v"(cd), "s"(kb)); \
            us2 v; v.x = *(LAS const u16*)(L + a0); v.y = *(LAS const u16*)(L + a1); \
            d[i] = __builtin_bit_cast(u32, v); \
        } \
        if (m == 0) { d[0] = 0u; d[1] = 0u; } \
        asm volatile("" : "+v"(d[0]), "+v"(d[1]), "+v"(d[2]), "+v"(d[3]), "+v"(d[4]), "+v"(d[5])); \
        SB(); } while (0)
#define EXPAND2() do { SB(); write_copies(kcw, xd); lds_bar(); SB(); load_afrags(af, abase); SB(); } while (0)
#define TK(c) ((tpc * (u32)(c)) >> 16)
#define HPUT(e, id, val) do { const u32 hv_ = (val); __builtin_amdgcn_raw_buffer_store_b32(hv_, box, hint_w, (e) * 8192 + (id) * 4, STA); FENCE(); } while (0)
#define HB(e) (hb + (e) * 8192)
#define DUE(T, n) (~((n) - (T)) >> 31)
    {
        asm volatile("s_waitcnt vmcnt(0)" : "+v"(fl) :: "memory");
#pragma unroll 1
        for (u32 sp = 0; sp < (1u << 16); ++sp) {
            const unsigned long long bad = __builtin_amdgcn_ballot_w64(fl.x != nonce.x || fl.y != nonce.y);
            if (!bad) break;
            unsigned long long sv;
            asm volatile("s_sleep 1\n\ts_mov_b64 %1, exec\n\ts_mov_b64 exec, %4\n\tglobal_load_dwordx4 %0, %2, %3 " LDP "\n\ts_mov_b64 exec, %1\n\ts_waitcnt vmcnt(0)"
                         : "+v"(fl), "=&s"(sv) : "v"(flag_o), "s"(flag_b), "s"(bad) : "memory");
        }
        asm volatile("" : "+v"(q16));
        pc = box_req(gu, get_p);
        FENCE();
    }
    lds_bar();
#define KGEN2() do { if (w < 6) { \
        const int ch = w >= 3; \
        LAS const unsigned char* tbb = L + L_TB + ch * TB_S + (41 + n16) * TB_R + 16 * g; \
        v4u bfr[8]; \
        _Pragma("unroll") for (int s = 0; s < 8; ++s) bfr[s] = *(LAS const v4u*)(tbb + 64 * s); \
        const int mt = w - 3 * ch; \
        KGEN_MM(ch, mt, bfr); \
    } SB(); } while (0)
    KGEN2();
#define TPC(c0, r0, lo) do { \
        c_n = now(); r_n = rtime(); \
        const float q_ = (float)(r_n - (r0)) * 65536.f * __builtin_amdgcn_rcpf((float)(c_n - (c0))); \
        tpc = __builtin_amdgcn_readfirstlane((u32)fminf(fmaxf(q_, lo), 4400.f)); } while (0)
    u32 c_n, r_n;
    u32 tpc;
    {
        TPC(c_s, r_s, TPC_MIN);
    }
    SB();
    const float xc = lo2f((u32)__builtin_amdgcn_readfirstlane(q16.x));
    f2 x[4] = {up2(q16.x) - sp2(xc), up2(q16.y) - sp2(xc), up2(q16.z) - sp2(xc), up2(q16.w) - sp2(xc)};
    f2 gate[4];
    v4u hy, hu;
    u32 tq, rq0, nw;
#define LN_Q() do { \
            float rs0, nb0; \
            ln_stats(x, xc, 1.0f, rs0, nb0); \
            _Pragma("unroll") for (int i = 0; i < 4; ++i) gate[i] = fma2(x[i], sp2(rs0), sp2(nb0)); \
            asm volatile("" : "+v"(gate[0]), "+v"(gate[1]), "+v"(gate[2]), "+v"(gate[3]), "+v"(x[0]), "+v"(x[1]), "+v"(x[2]), "+v"(x[3])); \
            SB(); } while (0)
#define LNQ_WC LN_Q()
#define Y0_EARLY(BOXI) do { \
            tq = hint_chk((BOXI) == 1 ? 2 : 0, hy, rq0); \
            nw = DUE(tq, rq0); \
            py = sent4; py2 = sent4; \
            box_req_if(py, gy + (BOXI) * MB2, get_p, nw); box_req_if(py2, gy + (BOXI) * MB2 + 8192, get_p, nw); \
            SB(); } while (0)
    {
        asm volatile("s_waitcnt vmcnt(0)" : "+v"(pc) :: "memory");
        *(LAS v4u*)ust = pc;
        box_put(sent4, box, ini_y, 3 * MB2);
        lds_bar();
        if constexpr (WC) HPUT(0, wg, rtime() + TK(C_E0A) + T_VIS);
        load_b<0>(bq, bbase);
        const v2u i0 = *(LAS const v2u*)(urd), i1 = *(LAS const v2u*)(urd + U_S);
        ZERO_ACC();
        SB();
        conv_diag<true, 0, 0>(af, bq, acc, pbase);
        conv_diag<true, 0, 1>(af, bq, acc, pbase);
        f4 e0 = (f4){lo2f(i0.x), hi2f(i0.x), lo2f(i0.y), hi2f(i0.y)} * sp4(lo2f(dA2));
        f4 e1 = (f4){lo2f(i1.x), hi2f(i1.x), lo2f(i1.y), hi2f(i1.y)} * sp4(hi2f(dA2));
        asm volatile("" : "+v"(e0), "+v"(e1));
        SB();
        put_tile(acc[3], pbase, 3);
        if constexpr (!WC) {
            __builtin_amdgcn_s_setprio(3);
            lds_bar();
            EPILOGUE(1, 1.0f, 1.0f, 0);
            if constexpr (!HH) { hy = hint_req(HB(0), ho0); FENCE(); }
        } else {
            lds_bar();
            HPUT(1, wg, rtime() + TK(C_A1) + T_VIS);
            LNQ_WC;
        }
        v4u b2[2][4];
        conv_off<true, 0>(af, bq, acc);
        box_put(sent4, box, ini_u, 4 * MB2);
        FENCE();
        load_b<4>(b2, bbase);
        conv_off<true, 1>(af, bq, acc);
        box_put(sent4, box, ini_y, 5 * MB2);
        FENCE();
        lds_bar();
        if constexpr (WC && !HH) HPUT(2, rbk, rtime() + TK(C_UA) + T_VIS);
#define Y0_TRY(BOXI) do { \
            u32 c = 0; \
            if (!nw) { rq0 = rtime(); c = DUE(tq, rq0); } \
            box_req_if(py, gy + (BOXI) * MB2, get_p, c); box_req_if(py2, gy + (BOXI) * MB2 + 8192, get_p, c); \
            nw |= c; SB(); } while (0)
#define Y0_LATE(E, BOXI) do { \
            if (!nw) hint_wait(tq, rq0, HB(E), ho0); \
            box_req_if(py, gy + (BOXI) * MB2, get_p, nw ^ 1); box_req_if(py2, gy + (BOXI) * MB2 + 8192, get_p, nw ^ 1); \
            SB(); } while (0)
        conv_diag<true, 4, 0>(af, b2, acc, pbase);
        if constexpr (!HH && !WC) Y0_EARLY(1);
        conv_diag<true, 4, 1>(af, b2, acc, pbase);
        if constexpr (HH) { hy = hint_req(HB(1), hoy); FENCE(); }
        put_tile(acc[7], pbase, 7);
        if constexpr (WC) __builtin_amdgcn_s_setprio(3);
        lds_bar();
        if constexpr (WC) EPILOGUE(1, 1.0f, 1.0f, 1);
        else { if constexpr (!HH) Y0_LATE(0, 1); LN_Q(); }
    }
    const float sc0 = __builtin_amdgcn_ldexpf(1.0f / USCALE, *(LAS const int*)(L + L_KCMP + 2824) - 7);
    const float sc1 = __builtin_amdgcn_ldexpf(1.0f / USCALE, *(LAS const int*)(L + L_KCMP + KCMP_S + 2824) - 7);

    v4u yAp;
#pragma unroll
    for (int it = 0; it < 3; ++it) {
        const int CR = it ? C_R1 : C_R0;
        const unsigned char* gub = gu + (2 + 2 * it) * MB2;
        {
            if (it == 1) {
                gate[0] = up2(yAp.x); gate[1] = up2(yAp.y); gate[2] = up2(yAp.z); gate[3] = up2(yAp.w);
                asm volatile("" : "+v"(gate[0]), "+v"(gate[1]), "+v"(gate[2]), "+v"(gate[3]));
            }
            if constexpr (HH) {
                if (WC && it == 0) { const u32 c0 = c_n, r0 = r_n; TPC(c0, r0, 2200.f); SB(); }
                if (it == 0) EXPAND1();
                tq = hint_chk(WC, hy, nw);
                hint_wait(tq, nw, HB(1 + 4 * it), hoy);
                py = box_req(gy + (1 + 2 * it) * MB2, get_p);
                if (it < 2) {
                    if constexpr (WC) HPUT(3 + 4 * it, rbk, rtime() + T_RD + TK(CR) + T_VIS);
                    else { hu = hint_req(HB(2 + 4 * it), ho); pc = sent4; }
                }
                if (it == 0) EXPAND2();
                py = box_fin(it < 2, py, gy + (1 + 2 * it) * MB2, get_p);
                PUT_TI(py, 0);
            } else if constexpr (!WC) {
                py = box_fin(1, py, gy + (1 + 2 * it) * MB2, get_p);
                PUT_TI(py, 0);
                py2 = box_fin(0, py2, gy + (1 + 2 * it) * MB2 + 8192, get_p);
                PUT_TI(py2, 128);
            } else if (it == 0) {
                asm volatile("s_waitcnt vmcnt(5)" ::: "memory");
            }
            lds_bar();
            const v4u yv = *(LAS const v4u*)(L + L_TI + w * 1040 + l * 16);
            SB();
            if (it < 2) {
                if constexpr (HH && !WC) {
                    tq = hint_chk(0, hu, rq0);
                    nw = DUE(tq, rq0);
                    box_req_if(pc, gub, get_p, nw);
                    SB();
                }
            }
#define U0_TRY() do { \
                    u32 c = 0; \
                    if (!nw) { rq0 = rtime(); c = DUE(tq, rq0); } \
                    box_req_if(pc, gub, get_p, c); \
                    nw |= c; SB(); } while (0)
            const f2 yc[4] = {up2(yv.x), up2(yv.y), up2(yv.z), up2(yv.w)};
            float rs, nb;
            if (it == 0) {
#pragma unroll
                for (int i = 0; i < 4; ++i) x[i] = fma2(gate[i], yc[i], x[i]);
                yAp = yv;
            } else {
#pragma unroll
                for (int i = 0; i < 4; ++i) x[i] += yc[i];
            }
            if (it == 2) {
                const __amdgpu_buffer_rsrc_t ob = __builtin_amdgcn_make_buffer_rsrc(out, 0, SEQ * DM * 2, 0x00020000);
                v4u o = {pk_bf(x[0].x, x[0].y), pk_bf(x[1].x, x[1].y), pk_bf(x[2].x, x[2].y), pk_bf(x[3].x, x[3].y)};
                __builtin_amdgcn_raw_buffer_store_b128(o, ob, (u32)(row * DM * 2 + 16 * l), 0, 17);
                break;
            }
            if (it == 1) {
                ln_stats(x, xc, 1.0f, rs, nb);
                if constexpr (HH && !WC) { SB(); U0_TRY(); }
#pragma unroll
                for (int i = 0; i < 4; ++i) x[i] = fma2(fma2(x[i], sp2(rs), sp2(nb)), gate[i], x[i]);
            }
            ln_stats(x, xc, USCALE, rs, nb);
            v4u uo;
#pragma unroll
            for (int i = 0; i < 4; ++i) { const f2 u = fma2(x[i], sp2(rs), sp2(nb)); uo[i] = pk_h(u.x, u.y); }
            *(LAS v4u*)(L + L_TO + w * 1088 + l * 16) = uo;
            if constexpr (!WC) {
                SB();
                if constexpr (HH) U0_TRY();
                else hu = hint_req(HB(2 + 4 * it), ho);
                SB();
            } else if constexpr (HH) { SB(); hu = hint_req(HB(3 + 4 * it), hou); SB(); }
            lds_bar();
            const v2u lo2 = tr_read(tr), hi2 = tr_read(tr + 4 * 1088);
            box_put((v4u){clampS(lo2.x), clampS(lo2.y), clampS(hi2.x), clampS(hi2.y)}, box, pub_row, (2 + 2 * it) * MB2);
            FENCE();
            if (it == 1) {
#pragma unroll
                for (int i = 0; i < 4; ++i) x[i] += sp2(xc);
                asm volatile("" : "+v"(x[0]), "+v"(x[1]), "+v"(x[2]), "+v"(x[3]));
            }
            if (WC && !HH && it == 0) { const u32 c0 = c_n, r0 = r_n; TPC(c0, r0, 2200.f); SB(); }
        }
        {
            if constexpr (HH) {
                if constexpr (!WC) {
                    if (!nw) hint_wait(tq, rq0, HB(2 + 4 * it), ho);
                    box_req_if(pc, gub, get_p, nw ^ 1);
                    if (!nw) vm_wait();
                    pc = box_fin(1, pc, gub, get_p);
                    *(LAS v4u*)ust = pc;
                }
            } else {
                if constexpr (WC) rq0 = rtime();
                if (it == 0) EXPAND1();
                if constexpr (!WC) {
                    tq = hint_chk(1, hu, nw);
                    hint_wait(tq, nw, HB(2 + 4 * it), ho);
                    pc = box_req(gub, get_p);
                }
                if (it == 0) EXPAND2();
                if constexpr (!WC) { pc = box_fin(0, pc, gub, get_p); *(LAS v4u*)ust = pc; }
                else { wait_rt(rq0 + T_H1); tq = hint_chk(0, hint_req(HB(3 + 4 * it), hou), nw); }
            }
            lds_bar();
            u32 ts;
            if constexpr (WC) {
                ts = rtime();
                HPUT(4 + 4 * it, wg, ts + TK(C_E0) + T_VIS);
                pc = sent4;
                if constexpr (!HH) { rq0 = ts; nw = DUE(tq, ts); box_req_if(pc, gub, get_p, nw); SB(); }
            }
            load_b<0>(bq, bbase);
            const float d0 = lo2f(dB2) * (1.0f / USCALE), d1 = hi2f(dB2) * (1.0f / USCALE);
#define SKIP_LOAD() const v2u i0 = *(LAS const v2u*)(urd), i1 = *(LAS const v2u*)(urd + U_S)
#define SKIP_TERMS() \
            const h2 h00 = as_h2(i0.x), h01 = as_h2(i0.y), h10 = as_h2(i1.x), h11 = as_h2(i1.y); \
            f4 e0 = (f4){(float)h00.x, (float)h00.y, (float)h01.x, (float)h01.y} * sp4(d0); \
            f4 e1 = (f4){(float)h10.x, (float)h10.y, (float)h11.x, (float)h11.y} * sp4(d1); \
            asm volatile("" : "+v"(e0), "+v"(e1)); SB()
            ZERO_ACC();
            if constexpr (!WC) {
                SKIP_LOAD();
                SB();
                conv_diag<false, 0, 0>(af, bq, acc, pbase);
                conv_diag<false, 0, 1>(af, bq, acc, pbase);
                SKIP_TERMS();
                put_tile(acc[3], pbase, 3);
                __builtin_amdgcn_s_setprio(3);
                lds_bar();
                EPILOGUE(3 + 2 * it, sc0, sc1, 4 + 4 * it);
            } else {
                SB();
                conv_diag<false, 0, 0>(af, bq, acc, pbase);
                if constexpr (HH) { tq = hint_chk(2, hu, rq0); nw = DUE(tq, rq0); box_req_if(pc, gub, get_p, nw); SB(); }
                else U0_TRY();
                conv_diag<false, 0, 1>(af, bq, acc, pbase);
                put_tile(acc[3], pbase, 3);
                lds_bar();
                if (!nw) { hint_wait(tq, rq0, HB(3 + 4 * it), hou); rq0 = rtime(); }
                box_req_if(pc, gub, get_p, nw ^ 1);
                ts += TK(C_01);
                HPUT(5 + 4 * it, wg, ts + max(0, (int)(rq0 + T_RD - ts)) + TK(C_E1) + T_VIS);
            }
            if (it == 0) {
                __builtin_amdgcn_raw_buffer_store_b64(~nonce, box, flag_w, 0, 16);
                FENCE();
            }
            if constexpr (!HH && !WC) { hy = hint_req(HB(4 + 4 * it), ho0); FENCE(); }
            conv_off<false, 0>(af, bq, acc);
            if constexpr (WC) { __builtin_amdgcn_s_sleep(0); SB(); }
            conv_off<false, 1>(af, bq, acc);
            if constexpr (WC) {
                pc = box_fin(1 + (it == 0), pc, gub, get_p);
                *(LAS v4u*)ust = pc;
            }
            lds_bar();
            if (WC && !HH && it == 0) HPUT(6, rbk, rtime() + TK(C_UB) + T_VIS);
            load_b<4>(bq, bbase);
            if constexpr (WC) {
                SKIP_LOAD();
                SB();
                conv_diag<false, 4, 0>(af, bq, acc, pbase);
                conv_diag<false, 4, 1>(af, bq, acc, pbase);
                SKIP_TERMS();
                if constexpr (HH) { hy = hint_req(HB(5 + 4 * it), hoy); FENCE(); }
                put_tile(acc[7], pbase, 7);
                __builtin_amdgcn_s_setprio(3);
                lds_bar();
                EPILOGUE(3 + 2 * it, sc0, sc1, 5 + 4 * it);
            } else {
                conv_diag<false, 4, 0>(af, bq, acc, pbase);
                if constexpr (!HH) Y0_EARLY(3 + 2 * it);
                conv_diag<false, 4, 1>(af, bq, acc, pbase);
                if constexpr (HH) { hy = hint_req(HB(5 + 4 * it), hoy); FENCE(); }
                put_tile(acc[7], pbase, 7);
                lds_bar();
                if constexpr (!HH) Y0_LATE(4 + 4 * it, 3 + 2 * it);
            }
        }
    }
    };
    if (H) { if (w < 4) body(BC<true>{}, BC<false>{}); else body(BC<true>{}, BC<true>{}); }
    else   { if (w < 4) body(BC<false>{}, BC<false>{}); else body(BC<false>{}, BC<true>{}); }
}

extern "C" void kernel_launch(void* const* d_in, const int* in_sizes, int n_in, void* d_out, int out_size, void* d_ws, size_t ws_size,
                              hipStream_t stream) {
    (void)hipFuncSetAttribute((const void*)k1_main, hipFuncAttributeMaxDynamicSharedMemorySize, L_END);
    k1_main<<<256, 512, L_END, stream>>>((const u16*)d_in[0], (const u16*)d_in[1], (const u16*)d_in[2], (const u16*)d_in[3], (const u16*)d_in[4],
                                         (const u16*)d_in[5], (const u16*)d_in[7], (const u16*)d_in[8], (const u16*)d_in[9], (const u16*)d_in[10],
                                         (const u16*)d_in[11], (const u16*)d_in[12], (u16*)d_out, (unsigned char*)d_ws);
}
```
